```python
import torch

def _softmax(x, axis=-1):
    return torch.softmax(x, dim=axis)

def _gelu(x):
    return torch.nn.functional.gelu(x, approximate='tanh')

def _silu(x):
    return torch.nn.functional.silu(x)

def _sigmoid(x):
    return torch.sigmoid(x)

def _layernorm(x, axis=-1, eps=1e-05):
    m = x.mean(dim=axis, keepdim=True)
    v = x.var(dim=axis, keepdim=True, unbiased=False)
    return (x - m) / torch.sqrt(v + eps)

def _scatter_add(tgt, idx, src, axis=0):
    return tgt.clone().index_add_(axis, idx, src)

def _segment_sum(src, seg, n):
    out = torch.zeros((n,) + src.shape[1:], dtype=src.dtype)
    return out.index_add_(0, seg, src)

def _pad(x, pw):
    flat = tuple((v for pair in reversed(pw) for v in pair))
    return torch.nn.functional.pad(x, flat)
kernel_name = 'adaLN_cond__ssm_conv_h3__glu_gated_reglu__prompt'

def setup_inputs():

    _w = lambda a: (lambda t: t.to(torch.bfloat16) if t.is_floating_point() else t)(torch.as_tensor(a))

    def _A_setup():
        B, S, D = (1, 2048, 576)
        ks = 4
        return {'q': _w((lambda *s: torch.randn(*s, dtype=torch.bfloat16) * 0.05)(B, S, D)), 'k': _w((lambda *s: torch.randn(*s, dtype=torch.bfloat16) * 0.8)(B, S, D)), 'v': _w((lambda *s: torch.randn(*s, dtype=torch.bfloat16) * 0.8)(B, S, D)), 'shift_k': _w((lambda *s: torch.randn(*s, dtype=torch.bfloat16) * 0.8)(D, ks) * 0.5), 'ssm_k': _w((lambda *s: torch.randn(*s, dtype=torch.bfloat16) * 0.8)(D, S) * (1.0 / S ** 0.5)), 'ssm_D': _w((lambda *s: torch.randn(*s, dtype=torch.bfloat16) * 0.8)(D))}
    _A_in = _A_setup()

    def _B_setup():
        B, S, D, F = (1, 2048, 576, 4608)
        return {'x': _w((lambda *s: torch.randn(*s, dtype=torch.bfloat16) * 0.05)(B, S, D)), 'W_gate': _w((lambda *s: torch.randn(*s, dtype=torch.bfloat16) * 0.8)(D, F) * (2.0 / D) ** 0.5), 'W_up': _w((lambda *s: torch.randn(*s, dtype=torch.bfloat16) * 0.8)(D, F) * (2.0 / D) ** 0.5), 'W_down': _w((lambda *s: torch.randn(*s, dtype=torch.bfloat16) * 0.8)(F, D) * (1.0 / F) ** 0.5)}
    _B_in = _B_setup()
    return {'A_q': _A_in['q'], 'A_k': _A_in['k'], 'A_v': _A_in['v'], 'A_shift_k': _A_in['shift_k'], 'A_ssm_k': _A_in['ssm_k'], 'A_ssm_D': _A_in['ssm_D'], 'B_x': _B_in['x'], 'B_W_gate': _B_in['W_gate'], 'B_W_up': _B_in['W_up'], 'B_W_down': _B_in['W_down'], 'c': _w((lambda *s: torch.randn(*s, dtype=torch.bfloat16) * 0.2)(1, 576) * 8.0), 'w_mod': _w((lambda *s: torch.randn(*s, dtype=torch.bfloat16) * 0.2)(576, 6 * 576) * 576 ** (-0.5) * 8.0)}

def reference(A_q, A_k, A_v, A_shift_k, A_ssm_k, A_ssm_D, B_x, B_W_gate, B_W_up, B_W_down, c, w_mod):

    def _A_ref(q, k, v, shift_k, ssm_k, ssm_D):
        B, S, D = q.shape
        ks = shift_k.shape[-1]
        k_pad = _pad(k, ((0, 0), (ks - 1, 0), (0, 0)))
        k_shift = torch.zeros_like(k)
        for t in range(ks):
            k_shift = k_shift + k_pad[:, t:t + S, :] * shift_k[:, ks - 1 - t][None, None, :]
        kv = k_shift * v
        L = 2 * S
        kv_pad = _pad(kv, ((0, 0), (0, S), (0, 0)))
        kf_pad = _pad(ssm_k, ((0, 0), (0, S)))
        kv_f = (lambda x, axes=(-2, -1): torch.fft.rfft2(x.float(), dim=axes))(kv_pad[:, None, :, :], axes=(1, 2))
        kf_f = (lambda x, axes=(-2, -1): torch.fft.rfft2(x.float(), dim=axes))(kf_pad[:, None, :], axes=(1, 2))
        kf_f = (lambda x, *d: x.permute(*d))(kf_f, 1, 2, 0)
        y_f = kv_f * kf_f[None, :, :, :]
        y = (lambda x, s, axes=(-2, -1): torch.fft.irfft2(x, s=s, dim=axes).to(torch.bfloat16))(y_f, (1, L), axes=(1, 2))[:, 0, :S, :]
        y = y + kv * ssm_D[None, None, :]
        return q * y

    def _B_ref(x, W_gate, W_up, W_down):
        gate = torch.einsum('bsd,df->bsf', x, W_gate)
        up = torch.einsum('bsd,df->bsf', x, W_up)
        gate = gate * (lambda x, dt: x.to(dt))(gate > 0, gate.dtype)
        h = gate * up
        return torch.einsum('bsf,fd->bsd', h, W_down)
    x = A_q
    mod = (_silu(c) @ w_mod).reshape(c.shape[0], 6, 1, -1)
    sa, ca, ga, sb, cb, gb = (mod[:, 0].clone(), mod[:, 1].clone(), mod[:, 2].clone(), mod[:, 3].clone(), mod[:, 4].clone(), mod[:, 5].clone())
    h = _layernorm(x, axis=-1) * (1 + ca) + sa
    x = x + ga * _A_ref(h, A_k, A_v, A_shift_k, A_ssm_k, A_ssm_D)
    h = _layernorm(x, axis=-1) * (1 + cb) + sb
    x = x + gb * _B_ref(h, B_W_gate, B_W_up, B_W_down)
    return x
```

```cpp
#include <hip/hip_runtime.h>
#include <hip/hip_bf16.h>
#include <stdint.h>

typedef __attribute__((ext_vector_type(8))) short bf16x8;
typedef __attribute__((ext_vector_type(4))) float f32x4;
typedef __attribute__((ext_vector_type(2))) float f32x2;
typedef __attribute__((ext_vector_type(16))) float f32x16;
typedef __attribute__((ext_vector_type(4))) unsigned u32x4;
typedef __attribute__((ext_vector_type(2))) unsigned u32x2;
typedef __attribute__((ext_vector_type(2))) __bf16 bf16x2_t;
typedef unsigned short u16;

#define DI __device__ __forceinline__

constexpr int S_ = 2048, D_ = 576, F_ = 4608;

constexpr size_t WS_MOD  = 0;
constexpr size_t WS_Y    = 16384;
constexpr size_t WS_X1   = WS_Y + (size_t)S_ * D_ * 2;
constexpr size_t WS_XF   = WS_X1 + (size_t)S_ * D_ * 2;
constexpr size_t WS_WGU  = WS_XF + (size_t)S_ * D_ * 2;
constexpr size_t WS_WD   = WS_WGU + (size_t)D_ * F_ * 2 * 2;
constexpr size_t WS_HF   = WS_WD + (size_t)D_ * F_ * 2;
constexpr size_t WS_PART = WS_HF + (size_t)S_ * F_ * 2;
constexpr size_t WS_END  = WS_PART + (size_t)4 * S_ * D_ * 4;

DI float bf2f(unsigned u) { return __uint_as_float(u << 16); }
DI float bflo(unsigned u) { return __uint_as_float(u << 16); }
DI float bfhi(unsigned u) { return __uint_as_float(u & 0xffff0000u); }
DI unsigned pack2(float a, float b) {
  f32x2 v = {a, b};
  bf16x2_t r = __builtin_convertvector(v, bf16x2_t);
  return __builtin_bit_cast(unsigned, r);
}
DI float rbf(float a) { return bflo(pack2(a, 0.f) & 0xffffu); }

constexpr int NCONV = 144, NMOD = 54;
constexpr int KC_STRIDE = 4160 + 64;
constexpr int KCS = 4160;
constexpr int KER_BYTES = 4 * KCS;
constexpr int RS = 4608;
constexpr int CONV_LDS = 4 * KER_BYTES + 4 * RS;

DI void transpose8x8(const u32x4 (&r)[8], u32x4 (&c)[8]) {
#pragma unroll
  for (int cp = 0; cp < 4; ++cp) {
#pragma unroll
    for (int p = 0; p < 4; ++p) {
      unsigned a = r[2 * p][cp], b = r[2 * p + 1][cp];
      c[2 * cp][p]     = __builtin_amdgcn_perm(b, a, 0x05040100u);
      c[2 * cp + 1][p] = __builtin_amdgcn_perm(b, a, 0x07060302u);
    }
  }
}

DI void prep_wgu_unit(int unit, int lane, const u16* Wg, const u16* Wu, char* ws) {
  int mat = unit / (72 * 9), rem = unit % (72 * 9);
  int kg = rem / 9, fs = rem % 9;
  const u16* W = mat ? Wu : Wg;
  int f0 = (fs * 64 + lane) * 8;
  u32x4 r[8], c[8];
#pragma unroll
  for (int j = 0; j < 8; ++j) r[j] = *(const u32x4*)(W + (size_t)(kg * 8 + j) * F_ + f0);
  transpose8x8(r, c);
  int ks = kg >> 1, h = kg & 1;
  int ft = f0 >> 4, cb = ft / 9, ftl = ft % 9;
  char* dst = ws + WS_WGU + ((size_t)(cb * 36 + ks) * 9 + ftl) * 1024 + (h * 32 + mat * 16 + (f0 & 15)) * 16;
#pragma unroll
  for (int j = 0; j < 8; ++j) *(u32x4*)(dst + j * 16) = c[j];
}
DI void prep_wd_thread(int tid, const u16* Wd, char* ws) {
  int dg = tid % 72, rem = tid / 72;
  int g = rem & 3, k32 = rem >> 2;
  int fb = 32 * k32 + 16 * (g >> 1) + 4 * (g & 1);
  u32x4 r[8], c[8];
#pragma unroll
  for (int j = 0; j < 8; ++j) r[j] = *(const u32x4*)(Wd + (size_t)(fb + 8 * (j >> 2) + (j & 3)) * D_ + dg * 8);
  transpose8x8(r, c);
  int d0 = dg * 8, db = d0 / 144, dt = (d0 % 144) >> 4;
  char* dst = ws + WS_WD + ((size_t)(db * 144 + k32) * 9 + dt) * 1024 + (g * 16 + (d0 & 15)) * 16;
#pragma unroll
  for (int j = 0; j < 8; ++j) *(u32x4*)(dst + j * 16) = c[j];
}

constexpr int NPREP1 = 1296 / 4 / 6;
__global__ __launch_bounds__(256, 1)
void k_front(const u16* __restrict__ Ak, const u16* __restrict__ Av, const u16* __restrict__ shk,
             const u16* __restrict__ ssmk, const u16* __restrict__ ssmD, const u16* __restrict__ cvec,
             const u16* __restrict__ wmod, const u16* __restrict__ Wg, const u16* __restrict__ Wu,
             char* __restrict__ ws) {
  extern __shared__ __attribute__((aligned(16))) char smem[];
  const int tid = threadIdx.x, lane = tid & 63, wid = tid >> 6;
  const int bid = blockIdx.x;
  if (bid < NCONV) {
    const int grp = (bid & 7) * 18 + (bid >> 3);
    const int c0 = grp * 4;
    char* kerS = smem;
    char* rS = smem + 4 * KER_BYTES;
    const int s0 = tid * 8;
    u32x2 kr[11], vr[8];
#pragma unroll
    for (int i = 0; i < 11; ++i) {
      int s = s0 - 3 + i;
      u32x2 z = {0u, 0u};
      kr[i] = (s >= 0) ? *(const u32x2*)(Ak + (size_t)s * D_ + c0) : z;
    }
#pragma unroll
    for (int i = 0; i < 8; ++i) vr[i] = *(const u32x2*)(Av + (size_t)(s0 + i) * D_ + c0);
    {
      const u16* kerg = ssmk + (size_t)(c0 + wid) * S_;
      char* kb = kerS + wid * KER_BYTES;
#pragma unroll
      for (int i = 0; i < 4; ++i) {
        int ch = i * 64 + lane;
        u32x4 a = *(const u32x4*)(kerg + ch * 8);
        u32x2 b = {0u, 0u};
        if (ch < 255) b = *(const u32x2*)(kerg + ch * 8 + 8);
        u32x4 c1, c2, c3;
        c1[0] = __builtin_amdgcn_alignbit(a[1], a[0], 16); c1[1] = __builtin_amdgcn_alignbit(a[2], a[1], 16);
        c1[2] = __builtin_amdgcn_alignbit(a[3], a[2], 16); c1[3] = __builtin_amdgcn_alignbit(b[0], a[3], 16);
        c2[0] = a[1]; c2[1] = a[2]; c2[2] = a[3]; c2[3] = b[0];
        c3[0] = c1[1]; c3[1] = c1[2]; c3[2] = c1[3]; c3[3] = __builtin_amdgcn_alignbit(b[1], b[0], 16);
        int off = (ch * 8 + 16) * 2;
        *(u32x4*)(kb + off) = a;
        *(u32x4*)(kb + KCS + off) = c1;
        *(u32x4*)(kb + 2 * KCS + off) = c2;
        *(u32x4*)(kb + 3 * KCS + off) = c3;
        if (ch == 0) {
          u32x4 z = {0u, 0u, 0u, 0u};
          *(u32x4*)(kb) = z; *(u32x4*)(kb + KCS) = z; *(u32x4*)(kb + 2 * KCS) = z; *(u32x4*)(kb + 3 * KCS) = z;
          u32x4 e1 = z, e2 = z, e3 = z;
          e1[3] = a[0] << 16;
          e2[3] = a[0];
          e3[2] = a[0] << 16; e3[3] = c1[0];
          *(u32x4*)(kb + 16) = z;
          *(u32x4*)(kb + KCS + 16) = e1;
          *(u32x4*)(kb + 2 * KCS + 16) = e2;
          *(u32x4*)(kb + 3 * KCS + 16) = e3;
        }
      }
      if (lane < 32) { u32x4 z = {0u, 0u, 0u, 0u}; *(u32x4*)(rS + wid * RS + 4096 + lane * 16) = z; }
    }
    {
      float w[4][4];
#pragma unroll
      for (int c = 0; c < 4; ++c) {
        u32x2 t = *(const u32x2*)(shk + (c0 + c) * 4);
        w[c][0] = bflo(t[0]); w[c][1] = bfhi(t[0]); w[c][2] = bflo(t[1]); w[c][3] = bfhi(t[1]);
      }
#pragma unroll
      for (int c = 0; c < 4; ++c) {
        float kvv[8];
#pragma unroll
        for (int i = 0; i < 8; ++i) {
          float acc = 0.f;
#pragma unroll
          for (int t = 0; t < 4; ++t) {
            unsigned u = kr[i + t][c >> 1];
            float kk = (c & 1) ? bfhi(u) : bflo(u);
            float p = rbf(kk * w[c][3 - t]);
            acc = (t == 0) ? p : rbf(acc + p);
          }
          unsigned uv = vr[i][c >> 1];
          float vv = (c & 1) ? bfhi(uv) : bflo(uv);
          kvv[i] = acc * vv;
        }
        u32x4 o;
        o[0] = pack2(kvv[7], kvv[6]); o[1] = pack2(kvv[5], kvv[4]); o[2] = pack2(kvv[3], kvv[2]); o[3] = pack2(kvv[1], kvv[0]);
        *(u32x4*)(rS + c * RS + (2040 - s0) * 2) = o;
      }
    }
    __syncthreads();
    f32x4 acc[8];
#pragma unroll
    for (int t = 0; t < 8; ++t) acc[t] = (f32x4){0.f, 0.f, 0.f, 0.f};
    {
      const int b = lane & 15, g = lane >> 4;
      const int x0 = 1 + b + 8 * g;
      const char* abase = kerS + wid * KER_BYTES + (x0 & 3) * KCS + (x0 & ~3) * 2;
      const char* gbase = rS + wid * RS + (2032 - 16 * b + 8 * g) * 2;
#pragma unroll 2
      for (int u0 = 0; u0 < 8; ++u0) {
        bf16x8 G[8];
#pragma unroll
        for (int i = 0; i < 8; ++i) G[i] = *(const bf16x8*)(gbase + u0 * 64 - i * 512);
#pragma unroll
        for (int u1 = 0; u1 < 8; ++u1) {
          u32x2 lo = *(const u32x2*)(abase + u0 * 64 + u1 * 512);
          u32x2 hi = *(const u32x2*)(abase + u0 * 64 + u1 * 512 + 8);
          u32x4 av = {lo[0], lo[1], hi[0], hi[1]};
          bf16x8 A = __builtin_bit_cast(bf16x8, av);
#pragma unroll
          for (int t = u1; t < 8; ++t)
            acc[t] = __builtin_amdgcn_mfma_f32_16x16x32_bf16(A, G[t - u1], acc[t], 0, 0, 0);
        }
      }
      float Dv = bf2f(ssmD[c0 + wid]);
      char* yb = kerS + wid * KER_BYTES;
#pragma unroll
      for (int t = 0; t < 8; ++t) {
        int s = 256 * t + 16 * b + 4 * g;
        u32x2 kv = *(const u32x2*)(rS + wid * RS + (2047 - s - 3) * 2);
        float k3 = bflo(kv[0]), k2 = bfhi(kv[0]), k1 = bflo(kv[1]), k0 = bfhi(kv[1]);
        float y0 = rbf(acc[t][0]) + rbf(k0 * Dv), y1 = rbf(acc[t][1]) + rbf(k1 * Dv);
        float y2 = rbf(acc[t][2]) + rbf(k2 * Dv), y3 = rbf(acc[t][3]) + rbf(k3 * Dv);
        u32x2 o = {pack2(y0, y1), pack2(y2, y3)};
        *(u32x2*)(yb + s * 2) = o;
      }
    }
    __syncthreads();
    {
      u32x4 yy[4];
#pragma unroll
      for (int c = 0; c < 4; ++c) yy[c] = *(const u32x4*)(kerS + c * KER_BYTES + s0 * 2);
      u16* Y = (u16*)(ws + WS_Y);
#pragma unroll
      for (int i = 0; i < 8; ++i) {
        u32x2 o;
        unsigned sel = (i & 1) ? 0x07060302u : 0x05040100u;
        o[0] = __builtin_amdgcn_perm(yy[1][i >> 1], yy[0][i >> 1], sel);
        o[1] = __builtin_amdgcn_perm(yy[3][i >> 1], yy[2][i >> 1], sel);
        *(u32x2*)(Y + (size_t)(s0 + i) * D_ + c0) = o;
      }
    }
  } else if (bid < NCONV + NMOD) {
    const int nb = bid - NCONV;
    const int cg = tid & 7, rg = tid >> 3;
    float a[8];
#pragma unroll
    for (int j = 0; j < 8; ++j) a[j] = 0.f;
    u32x4 wv[18];
#pragma unroll
    for (int i = 0; i < 18; ++i) wv[i] = *(const u32x4*)(wmod + (size_t)(rg + 32 * i) * 3456 + nb * 64 + cg * 8);
#pragma unroll
    for (int i = 0; i < 18; ++i) {
      float cv = bf2f(cvec[rg + 32 * i]);
      float sv = rbf(cv / (1.f + __expf(-cv)));
#pragma unroll
      for (int p = 0; p < 4; ++p) { a[2 * p] += sv * bflo(wv[i][p]); a[2 * p + 1] += sv * bfhi(wv[i][p]); }
    }
#pragma unroll
    for (int j = 0; j < 8; ++j) {
      a[j] += __shfl_xor(a[j], 8); a[j] += __shfl_xor(a[j], 16); a[j] += __shfl_xor(a[j], 32);
    }
    float* red = (float*)smem;
    if (lane < 8) {
#pragma unroll
      for (int j = 0; j < 8; ++j) red[wid * 64 + lane * 8 + j] = a[j];
    }
    __syncthreads();
    if (tid < 64) {
      float r = red[tid] + red[64 + tid] + red[128 + tid] + red[192 + tid];
      ((float*)(ws + WS_MOD))[nb * 64 + tid] = rbf(r);
    }
  } else {
    const int pb = bid - NCONV - NMOD;
#pragma unroll 2
    for (int i = 0; i < 6; ++i) prep_wgu_unit((pb * 6 + i) * 4 + wid, lane, Wg, Wu, ws);
  }
}

constexpr int NROWB = 256;
constexpr int NPREP2 = 144 * 4 * 72 / 256;
__global__ __launch_bounds__(256)
void k_rows(const u16* __restrict__ Aq, const u16* __restrict__ Wd, char* __restrict__ ws) {
  const int tid = threadIdx.x, bid = blockIdx.x;
  if (bid >= NROWB) { prep_wd_thread((bid - NROWB) * 256 + tid, Wd, ws); return; }
  const int row = bid * 8 + (tid >> 5), c = tid & 31;
  const float* mod = (const float*)(ws + WS_MOD);
  const unsigned* q32 = (const unsigned*)(Aq + (size_t)row * D_);
  const unsigned* y32 = (const unsigned*)(ws + WS_Y) + (size_t)row * (D_ / 2);
  unsigned qv[9], yv[9];
#pragma unroll
  for (int i = 0; i < 9; ++i) { qv[i] = q32[c + 32 * i]; yv[i] = y32[c + 32 * i]; }
  float x[18];
  float sum = 0.f;
#pragma unroll
  for (int i = 0; i < 9; ++i) { x[2 * i] = bflo(qv[i]); x[2 * i + 1] = bfhi(qv[i]); sum += x[2 * i] + x[2 * i + 1]; }
#pragma unroll
  for (int o = 1; o < 32; o <<= 1) sum += __shfl_xor(sum, o);
  float mean = sum * (1.f / D_);
  float vs = 0.f;
#pragma unroll
  for (int i = 0; i < 18; ++i) { float d = x[i] - mean; vs += d * d; }
#pragma unroll
  for (int o = 1; o < 32; o <<= 1) vs += __shfl_xor(vs, o);
  float mb = rbf(mean), sd = rbf(sqrtf(rbf(rbf(vs * (1.f / D_)) + 1e-5f)));
  float x1[18];
  sum = 0.f;
#pragma unroll
  for (int i = 0; i < 9; ++i) {
    int ch = 2 * (c + 32 * i);
    f32x2 sa = *(const f32x2*)(mod + ch), ca = *(const f32x2*)(mod + D_ + ch), ga = *(const f32x2*)(mod + 2 * D_ + ch);
#pragma unroll
    for (int e = 0; e < 2; ++e) {
      float xv = x[2 * i + e];
      float n = rbf(rbf(xv - mb) / sd);
      float h = rbf(rbf(n * rbf(1.f + ca[e])) + sa[e]);
      float yy = e ? bfhi(yv[i]) : bflo(yv[i]);
      float a = rbf(h * yy);
      float r = rbf(xv + rbf(ga[e] * a));
      x1[2 * i + e] = r; sum += r;
    }
  }
#pragma unroll
  for (int o = 1; o < 32; o <<= 1) sum += __shfl_xor(sum, o);
  mean = sum * (1.f / D_);
  vs = 0.f;
#pragma unroll
  for (int i = 0; i < 18; ++i) { float d = x1[i] - mean; vs += d * d; }
#pragma unroll
  for (int o = 1; o < 32; o <<= 1) vs += __shfl_xor(vs, o);
  mb = rbf(mean); sd = rbf(sqrtf(rbf(rbf(vs * (1.f / D_)) + 1e-5f)));
  unsigned* x1o = (unsigned*)(ws + WS_X1) + (size_t)row * (D_ / 2);
  char* xf = ws + WS_XF + (size_t)(row >> 5) * 36 * 1024 + (row & 31) * 16;
#pragma unroll
  for (int i = 0; i < 9; ++i) {
    int u = c + 32 * i, ch = 2 * u;
    f32x2 sb = *(const f32x2*)(mod + 3 * D_ + ch), cb = *(const f32x2*)(mod + 4 * D_ + ch);
    float h[2];
#pragma unroll
    for (int e = 0; e < 2; ++e) {
      float n = rbf(rbf(x1[2 * i + e] - mb) / sd);
      h[e] = rbf(n * rbf(1.f + cb[e])) + sb[e];
    }
    x1o[u] = pack2(x1[2 * i], x1[2 * i + 1]);
    int chunk = u >> 2;
    *(unsigned*)(xf + chunk * 512 + (u & 3) * 4) = pack2(h[0], h[1]);
  }
}

#define SCHED0 __builtin_amdgcn_sched_barrier(0)
template <int N> DI void wait_vm() { asm volatile("s_waitcnt vmcnt(%0)" ::"i"(N) : "memory"); }

template <int OFF> DI void dma1k(const char* sbase, unsigned voff, unsigned ldsdst) {
  asm volatile("s_mov_b32 m0, %2\n\ts_nop 0\n\tglobal_load_lds_dwordx4 %0, %1 offset:%3"
               ::"v"(voff), "s"(sbase), "s"(ldsdst), "i"(OFF) : "memory");
}
template <int OFF> DI void gload16(bf16x8& dst, const char* sbase, unsigned voff) {
  asm volatile("global_load_dwordx4 %0, %1, %2 offset:%3" : "=v"(dst) : "v"(voff), "s"(sbase), "i"(OFF) : "memory");
}

DI void mfma32a(f32x16& c, const bf16x8& a, const bf16x8& b) {
  asm volatile("v_mfma_f32_32x32x16_bf16 %0, %1, %2, %0" : "+a"(c) : "v"(a), "v"(b));
}
DI void mfma32v(f32x16& c, const bf16x8& a, const bf16x8& b) {
  asm volatile("v_mfma_f32_32x32x16_bf16 %0, %1, %2, %0" : "+v"(c) : "v"(a), "v"(b));
}
constexpr int SLOT = 36 * 1024;
constexpr int GEMM_LDS = 3 * SLOT;

struct G1State {
  f32x16 acc[2][9];
  bf16x8 xr[4][2];
  bf16x8 wf[2][9];
};

template <int LW, int LX, int NV0, int NV1, int NV2, int NV3, int LAST>
DI void g1_super(G1State& st, const char* smem_rd, const char* smem_rd_next, const char* wsrc, unsigned wdst,
                 const char* xsrc, unsigned voff, unsigned xoff0, unsigned xoff1) {
#pragma unroll
  for (int i = 0; i < 4; ++i) {
    constexpr int NVs[4] = {NV0, NV1, NV2, NV3};
    const int cur = i & 1, nxt = cur ^ 1;
    if (i == 0) wait_vm<NV0>(); else if (i == 1) wait_vm<NV1>(); else if (i == 2) wait_vm<NV2>(); else wait_vm<NV3>();
    asm volatile("" : "+v"(st.xr[i][0]), "+v"(st.xr[i][1]));
    if (i == 3 && !LAST) {
      asm volatile("s_waitcnt lgkmcnt(0)" ::: "memory");
      __builtin_amdgcn_s_barrier();
    }
    SCHED0;
    const char* rd = (i == 3) ? smem_rd_next : smem_rd + (i + 1) * 9216;
#pragma unroll
    for (int f = 0; f < 9; ++f) {
      if (f < 8) mfma32a(st.acc[0][f], st.wf[cur][f], st.xr[i][0]); else mfma32v(st.acc[0][f], st.wf[cur][f], st.xr[i][0]);
      if (!(i == 3 && LAST)) st.wf[nxt][f] = *(const bf16x8*)(rd + f * 1024);
      SCHED0;
      if (f < 8) mfma32a(st.acc[1][f], st.wf[cur][f], st.xr[i][1]); else mfma32v(st.acc[1][f], st.wf[cur][f], st.xr[i][1]);
      if (LW) {
        const int first = (i == 0) ? 0 : 2 * i + 1, cnt = (i == 0) ? 3 : 2;
        if (f % 3 == 1 && f / 3 < cnt) {
          const int p = first + f / 3;
          if (p < 4) { if (p == 0) dma1k<0>(wsrc, voff, wdst); if (p == 1) dma1k<1024>(wsrc, voff, wdst); if (p == 2) dma1k<2048>(wsrc, voff, wdst); if (p == 3) dma1k<3072>(wsrc, voff, wdst); }
          else if (p < 8) { if (p == 4) dma1k<0>(wsrc + 4096, voff, wdst + 4096); if (p == 5) dma1k<1024>(wsrc + 4096, voff, wdst + 4096); if (p == 6) dma1k<2048>(wsrc + 4096, voff, wdst + 4096); if (p == 7) dma1k<3072>(wsrc + 4096, voff, wdst + 4096); }
          else dma1k<0>(wsrc + 8192, voff, wdst + 8192);
        }
      }
      SCHED0;
    }
    if (LX) {
      if (i == 0) { gload16<0>(st.xr[0][0], xsrc, xoff0); gload16<0>(st.xr[0][1], xsrc, xoff1); }
      if (i == 1) { gload16<1024>(st.xr[1][0], xsrc, xoff0); gload16<1024>(st.xr[1][1], xsrc, xoff1); }
      if (i == 2) { gload16<2048>(st.xr[2][0], xsrc, xoff0); gload16<2048>(st.xr[2][1], xsrc, xoff1); }
      if (i == 3) { gload16<3072>(st.xr[3][0], xsrc, xoff0); gload16<3072>(st.xr[3][1], xsrc, xoff1); }
    }
    SCHED0;
  }
}

__global__ __launch_bounds__(256, 1)
void k_gemm1(char* __restrict__ ws) {
  extern __shared__ __attribute__((aligned(16))) char smem[];
  const int tid = threadIdx.x, lane = tid & 63;
  const int wid = __builtin_amdgcn_readfirstlane(tid >> 6);
  const int bid = blockIdx.x;
  const int xcd = bid & 7, idx = bid >> 3;
  const int cb = xcd * 4 + (idx & 3), rb = idx >> 2;
  const unsigned lds0 = (unsigned)(size_t)smem;
  const char* wbase = ws + WS_WGU + (size_t)cb * 36 * 9216 + wid * 9216;
  const char* xbase = ws + WS_XF + (size_t)(rb * 8 + wid * 2) * 36 * 1024;
  const unsigned voff = lane * 16;
  const unsigned xoff0 = lane * 16, xoff1 = lane * 16 + 36 * 1024;
  const unsigned wdst0 = lds0 + wid * 9216;

  G1State st;
#pragma unroll
  for (int m = 0; m < 2; ++m)
#pragma unroll
    for (int f = 0; f < 9; ++f)
#pragma unroll
      for (int r = 0; r < 16; ++r) st.acc[m][f][r] = 0.f;

#define DMA9(SRC, DST) do { \
    dma1k<0>(SRC, voff, DST); dma1k<1024>(SRC, voff, DST); dma1k<2048>(SRC, voff, DST); dma1k<3072>(SRC, voff, DST); \
    dma1k<0>((SRC) + 4096, voff, (DST) + 4096); dma1k<1024>((SRC) + 4096, voff, (DST) + 4096); dma1k<2048>((SRC) + 4096, voff, (DST) + 4096); dma1k<3072>((SRC) + 4096, voff, (DST) + 4096); \
    dma1k<0>((SRC) + 8192, voff, (DST) + 8192); } while (0)
  DMA9(wbase, wdst0);
  gload16<0>(st.xr[0][0], xbase, xoff0); gload16<0>(st.xr[0][1], xbase, xoff1);
  gload16<1024>(st.xr[1][0], xbase, xoff0); gload16<1024>(st.xr[1][1], xbase, xoff1);
  gload16<2048>(st.xr[2][0], xbase, xoff0); gload16<2048>(st.xr[2][1], xbase, xoff1);
  gload16<3072>(st.xr[3][0], xbase, xoff0); gload16<3072>(st.xr[3][1], xbase, xoff1);
  DMA9(wbase + SLOT, wdst0 + SLOT);
  wait_vm<17>();
  __builtin_amdgcn_s_barrier();
  SCHED0;
  const char* rd0 = smem + lane * 16;
#pragma unroll
  for (int f = 0; f < 9; ++f) st.wf[0][f] = *(const bf16x8*)(rd0 + f * 1024);
  SCHED0;

  int slot = 0;
#pragma unroll 1
  for (int s = 0; s < 7; ++s) {
    int slot1 = (slot == 2) ? 0 : slot + 1;
    int slot2 = (slot1 == 2) ? 0 : slot1 + 1;
    g1_super<1, 1, 12, 13, 13, 13, 0>(st, rd0 + slot * SLOT, rd0 + slot1 * SLOT, wbase + (size_t)(s + 2) * SLOT,
                                     wdst0 + slot2 * SLOT, xbase + (s + 1) * 4096, voff, xoff0, xoff1);
    slot = slot1;
  }
  g1_super<0, 1, 12, 10, 8, 6, 0>(st, rd0 + 1 * SLOT, rd0 + 2 * SLOT, wbase, wdst0, xbase + 8 * 4096, voff, xoff0, xoff1);
  g1_super<0, 0, 6, 4, 2, 0, 1>(st, rd0 + 2 * SLOT, rd0, wbase, wdst0, xbase, voff, xoff0, xoff1);

  asm volatile("s_nop 15\n\ts_nop 15" ::: "memory");
  SCHED0;
  const int h = lane >> 5, ml = lane & 31;
#pragma unroll
  for (int m = 0; m < 2; ++m) {
    const int mt16 = (rb * 8 + wid * 2 + m) * 2 + (ml >> 4);
    char* hb = ws + WS_HF + (size_t)mt16 * 144 * 1024 + (h * 16 + (ml & 15)) * 16;
#pragma unroll
    for (int f = 0; f < 9; ++f) {
      const int ft = cb * 9 + f;
      u32x4 o;
#pragma unroll
      for (int p = 0; p < 4; ++p) {
        float g0 = fmaxf(rbf(st.acc[m][f][2 * p]), 0.f), g1 = fmaxf(rbf(st.acc[m][f][2 * p + 1]), 0.f);
        float u0 = rbf(st.acc[m][f][8 + 2 * p]), u1 = rbf(st.acc[m][f][8 + 2 * p + 1]);
        o[p] = pack2(g0 * u0, g1 * u1);
      }
      *(u32x4*)(hb + (ft >> 1) * 1024 + (ft & 1) * 512) = o;
    }
  }
}

struct G2State {
  f32x4 acc[2][9];
  bf16x8 xr[4][2];
  bf16x8 wf[2][9];
};

template <int LW, int LX, int NV0, int NV1, int NV2, int NV3, int LAST>
DI void g2_super(G2State& st, const char* smem_rd, const char* smem_rd_next, const char* wsrc, unsigned wdst,
                 const char* xsrc, unsigned voff, unsigned xoff0, unsigned xoff1) {
#pragma unroll
  for (int i = 0; i < 4; ++i) {
    const int cur = i & 1, nxt = cur ^ 1;
    if (i == 0) wait_vm<NV0>(); else if (i == 1) wait_vm<NV1>(); else if (i == 2) wait_vm<NV2>(); else wait_vm<NV3>();
    asm volatile("" : "+v"(st.xr[i][0]), "+v"(st.xr[i][1]));
    if (i == 3 && !LAST) {
      asm volatile("s_waitcnt lgkmcnt(0)" ::: "memory");
      __builtin_amdgcn_s_barrier();
    }
    SCHED0;
    const char* rd = (i == 3) ? smem_rd_next : smem_rd + (i + 1) * 9216;
#pragma unroll
    for (int f = 0; f < 9; ++f) {
      st.acc[0][f] = __builtin_amdgcn_mfma_f32_16x16x32_bf16(st.wf[cur][f], st.xr[i][0], st.acc[0][f], 0, 0, 0);
      if (!(i == 3 && LAST)) st.wf[nxt][f] = *(const bf16x8*)(rd + f * 1024);
      SCHED0;
      st.acc[1][f] = __builtin_amdgcn_mfma_f32_16x16x32_bf16(st.wf[cur][f], st.xr[i][1], st.acc[1][f], 0, 0, 0);
      if (LW) {
        const int first = (i == 0) ? 0 : 2 * i + 1, cnt = (i == 0) ? 3 : 2;
        if (f % 3 == 1 && f / 3 < cnt) {
          const int p = first + f / 3;
          if (p < 4) { if (p == 0) dma1k<0>(wsrc, voff, wdst); if (p == 1) dma1k<1024>(wsrc, voff, wdst); if (p == 2) dma1k<2048>(wsrc, voff, wdst); if (p == 3) dma1k<3072>(wsrc, voff, wdst); }
          else if (p < 8) { if (p == 4) dma1k<0>(wsrc + 4096, voff, wdst + 4096); if (p == 5) dma1k<1024>(wsrc + 4096, voff, wdst + 4096); if (p == 6) dma1k<2048>(wsrc + 4096, voff, wdst + 4096); if (p == 7) dma1k<3072>(wsrc + 4096, voff, wdst + 4096); }
          else dma1k<0>(wsrc + 8192, voff, wdst + 8192);
        }
      }
      SCHED0;
    }
    if (LX) {
      if (i == 0) { gload16<0>(st.xr[0][0], xsrc, xoff0); gload16<0>(st.xr[0][1], xsrc, xoff1); }
      if (i == 1) { gload16<1024>(st.xr[1][0], xsrc, xoff0); gload16<1024>(st.xr[1][1], xsrc, xoff1); }
      if (i == 2) { gload16<2048>(st.xr[2][0], xsrc, xoff0); gload16<2048>(st.xr[2][1], xsrc, xoff1); }
      if (i == 3) { gload16<3072>(st.xr[3][0], xsrc, xoff0); gload16<3072>(st.xr[3][1], xsrc, xoff1); }
    }
    SCHED0;
  }
}

__global__ __launch_bounds__(256, 1)
void k_gemm2(char* __restrict__ ws) {
  extern __shared__ __attribute__((aligned(16))) char smem[];
  const int tid = threadIdx.x, lane = tid & 63;
  const int wid = __builtin_amdgcn_readfirstlane(tid >> 6);
  const int bid = blockIdx.x;
  const int xcd = bid & 7, idx = bid >> 3;
  const int q = xcd >> 1;
  const int rb = (xcd & 1) * 8 + (idx >> 2);
  const int db = idx & 3;
  const unsigned lds0 = (unsigned)(size_t)smem;
  const char* wbase = ws + WS_WD + ((size_t)db * 144 + q * 36) * 9216 + wid * 9216;
  const char* xbase = ws + WS_HF + ((size_t)(rb * 8 + wid * 2) * 144 + q * 36) * 1024;
  const unsigned voff = lane * 16;
  const unsigned xoff0 = lane * 16, xoff1 = lane * 16 + 144 * 1024;
  const unsigned wdst0 = lds0 + wid * 9216;

  G2State st;
#pragma unroll
  for (int m = 0; m < 2; ++m)
#pragma unroll
    for (int f = 0; f < 9; ++f) st.acc[m][f] = (f32x4){0.f, 0.f, 0.f, 0.f};

  DMA9(wbase, wdst0);
  gload16<0>(st.xr[0][0], xbase, xoff0); gload16<0>(st.xr[0][1], xbase, xoff1);
  gload16<1024>(st.xr[1][0], xbase, xoff0); gload16<1024>(st.xr[1][1], xbase, xoff1);
  gload16<2048>(st.xr[2][0], xbase, xoff0); gload16<2048>(st.xr[2][1], xbase, xoff1);
  gload16<3072>(st.xr[3][0], xbase, xoff0); gload16<3072>(st.xr[3][1], xbase, xoff1);
  DMA9(wbase + SLOT, wdst0 + SLOT);
  wait_vm<17>();
  __builtin_amdgcn_s_barrier();
  SCHED0;
  const char* rd0 = smem + lane * 16;
#pragma unroll
  for (int f = 0; f < 9; ++f) st.wf[0][f] = *(const bf16x8*)(rd0 + f * 1024);
  SCHED0;

  int slot = 0;
#pragma unroll 1
  for (int s = 0; s < 7; ++s) {
    int slot1 = (slot == 2) ? 0 : slot + 1;
    int slot2 = (slot1 == 2) ? 0 : slot1 + 1;
    g2_super<1, 1, 12, 13, 13, 13, 0>(st, rd0 + slot * SLOT, rd0 + slot1 * SLOT, wbase + (size_t)(s + 2) * SLOT,
                                     wdst0 + slot2 * SLOT, xbase + (s + 1) * 4096, voff, xoff0, xoff1);
    slot = slot1;
  }
  g2_super<0, 1, 12, 10, 8, 6, 0>(st, rd0 + 1 * SLOT, rd0 + 2 * SLOT, wbase, wdst0, xbase + 8 * 4096, voff, xoff0, xoff1);
  g2_super<0, 0, 6, 4, 2, 0, 1>(st, rd0 + 2 * SLOT, rd0, wbase, wdst0, xbase, voff, xoff0, xoff1);

  char* pb = ws + WS_PART + ((((size_t)q * 64 + rb * 4 + db) * 4 + wid) * 18) * 1024 + lane * 16;
#pragma unroll
  for (int m = 0; m < 2; ++m)
#pragma unroll
    for (int f = 0; f < 9; ++f) *(f32x4*)(pb + (m * 9 + f) * 1024) = st.acc[m][f];
}

__global__ __launch_bounds__(256)
void k_reduce(const char* __restrict__ ws, u16* __restrict__ out) {
  const int gid = blockIdx.x * 256 + threadIdx.x;
  const int lane = gid & 63, fr = gid >> 6;
  const int mf = fr % 18, tw = fr / 18, wid = tw & 3, tile = tw >> 2;
  const int m = mf / 9, dt = mf % 9, rb = tile >> 2, db = tile & 3;
  const size_t qs = (size_t)64 * 4 * 18 * 1024;
  const char* p = ws + WS_PART + (size_t)gid * 16;
  f32x4 a = *(const f32x4*)p, b = *(const f32x4*)(p + qs), c = *(const f32x4*)(p + 2 * qs), d = *(const f32x4*)(p + 3 * qs);
  const int row = rb * 128 + wid * 32 + m * 16 + (lane & 15);
  const int col = db * 144 + dt * 16 + (lane >> 4) * 4;
  u32x2 x1 = *(const u32x2*)((const u16*)(ws + WS_X1) + (size_t)row * D_ + col);
  f32x4 gb = *(const f32x4*)((const float*)(ws + WS_MOD) + 5 * D_ + col);
  f32x4 s = (a + b) + (c + d);
  float o0 = bflo(x1[0]) + rbf(gb[0] * rbf(s[0])), o1 = bfhi(x1[0]) + rbf(gb[1] * rbf(s[1]));
  float o2 = bflo(x1[1]) + rbf(gb[2] * rbf(s[2])), o3 = bfhi(x1[1]) + rbf(gb[3] * rbf(s[3]));
  u32x2 o = {pack2(o0, o1), pack2(o2, o3)};
  *(u32x2*)(out + (size_t)row * D_ + col) = o;
}

extern "C" void kernel_launch(void* const* d_in, const int* in_sizes, int n_in, void* d_out, int out_size,
                              void* d_ws, size_t ws_size, hipStream_t stream) {
  const u16* Aq = (const u16*)d_in[0];
  const u16* Ak = (const u16*)d_in[1];
  const u16* Av = (const u16*)d_in[2];
  const u16* shk = (const u16*)d_in[3];
  const u16* ssmk = (const u16*)d_in[4];
  const u16* ssmD = (const u16*)d_in[5];
  const u16* Wg = (const u16*)d_in[7];
  const u16* Wu = (const u16*)d_in[8];
  const u16* Wd = (const u16*)d_in[9];
  const u16* cvec = (const u16*)d_in[10];
  const u16* wmod = (const u16*)d_in[11];
  char* ws = (char*)d_ws;
  (void)hipFuncSetAttribute((const void*)k_front, hipFuncAttributeMaxDynamicSharedMemorySize, CONV_LDS);
  (void)hipFuncSetAttribute((const void*)k_gemm1, hipFuncAttributeMaxDynamicSharedMemorySize, GEMM_LDS);
  (void)hipFuncSetAttribute((const void*)k_gemm2, hipFuncAttributeMaxDynamicSharedMemorySize, GEMM_LDS);
  k_front<<<NCONV + NMOD + NPREP1, 256, CONV_LDS, stream>>>(Ak, Av, shk, ssmk, ssmD, cvec, wmod, Wg, Wu, ws);
  k_rows<<<NROWB + NPREP2, 256, 0, stream>>>(Aq, Wd, ws);
  k_gemm1<<<256, 256, GEMM_LDS, stream>>>(ws);
  k_gemm2<<<256, 256, GEMM_LDS, stream>>>(ws);
  k_reduce<<<64 * 4 * 18 * 64 / 256, 256, 0, stream>>>(ws, (u16*)d_out);
}
```

```cpp
#include <hip/hip_runtime.h>
#include <hip/hip_bf16.h>
#include <stdint.h>
#include <type_traits>

typedef __attribute__((ext_vector_type(8))) short bf16x8;
typedef __attribute__((ext_vector_type(4))) short s16x4;
typedef __attribute__((ext_vector_type(4))) float f32x4;
typedef __attribute__((ext_vector_type(2))) float f32x2;
typedef __attribute__((ext_vector_type(16))) float f32x16;
typedef __attribute__((ext_vector_type(4))) unsigned u32x4;
typedef __attribute__((ext_vector_type(2))) unsigned u32x2;
typedef __attribute__((ext_vector_type(2))) __bf16 bf16x2_t;
typedef __attribute__((address_space(3))) s16x4* lds_s16x4p;
typedef __attribute__((address_space(3))) char* lds_cp;
typedef unsigned short u16;

#define DI __device__ __forceinline__

constexpr int S_ = 2048, D_ = 576, F_ = 4608;

constexpr size_t SD2 = (size_t)S_ * D_ * 2;
constexpr size_t WS_MOD  = 0;
constexpr size_t WS_MODB = 13824;
constexpr size_t WS_ST1  = 16384;
constexpr size_t WS_ST2  = 32768;
constexpr size_t WS_FLAG = 49152;
constexpr size_t WS_UF   = WS_FLAG + 65536;
constexpr size_t WS_KVR  = WS_UF + 288 * 1024;
constexpr size_t WS_QT   = WS_KVR + SD2;
constexpr size_t WS_XF   = WS_QT + SD2;
constexpr size_t WS_X1F  = WS_XF + SD2;
constexpr size_t WS_HF   = WS_X1F + SD2;
constexpr size_t WS_PART = WS_HF + (size_t)S_ * F_ * 2;
constexpr size_t WS_END  = WS_PART + (size_t)4 * S_ * D_ * 4;

DI float bf2f(unsigned u) { return __uint_as_float(u << 16); }
DI float bflo(unsigned u) { return __uint_as_float(u << 16); }
DI float bfhi(unsigned u) { return __uint_as_float(u & 0xffff0000u); }
DI unsigned pack2(float a, float b) {
  f32x2 v = {a, b};
  bf16x2_t r = __builtin_convertvector(v, bf16x2_t);
  return __builtin_bit_cast(unsigned, r);
}
DI float rbf(float a) { return bflo(pack2(a, 0.f)); }
DI float dot2(unsigned a, unsigned b, float c) {
  return __builtin_amdgcn_fdot2_f32_bf16(__builtin_bit_cast(bf16x2_t, a), __builtin_bit_cast(bf16x2_t, b), c, false);
}
DI u32x2 trread(unsigned addr) {
  return __builtin_bit_cast(u32x2, __builtin_amdgcn_ds_read_tr16_b64_v4i16((lds_s16x4p)(size_t)addr));
}
template <int I, int N, typename Fn> DI void static_for(Fn&& fn) {
  if constexpr (I < N) { fn(std::integral_constant<int, I>{}); static_for<I + 1, N>(fn); }
}
#define SCHED0 __builtin_amdgcn_sched_barrier(0)
template <typename T> DI T* gpin(T* p) {
  unsigned long long v = (unsigned long long)p;
  asm volatile("" : "+s"(v));
  return (T*)(__attribute__((address_space(1))) T*)v;
}
template <int N> DI void wait_vm() { asm volatile("s_waitcnt vmcnt(%0)" ::"i"(N) : "memory"); }
template <int OFF> DI void dma1k(const char* sbase, unsigned voff, unsigned ldsdst) {
  asm volatile("s_add_u32 m0, %2, %3\n\ts_nop 0\n\tglobal_load_lds_dwordx4 %0, %1"
               ::"v"(voff), "s"(sbase), "s"(ldsdst), "i"(OFF) : "memory", "scc");
}

#define SYMADDR(V, SYM) asm volatile("s_getpc_b64 vcc\n\ts_add_u32 vcc_lo, vcc_lo, " SYM "@rel32@lo+4\n\t" \
    "s_addc_u32 vcc_hi, vcc_hi, " SYM "@rel32@hi+12\n\ts_mov_b64 %0, vcc" : "=s"(V) :: "vcc", "scc")
#define CODEPF(DST, SYM, END, CHUNK, LANE) do { unsigned long long a_, e_; SYMADDR(a_, SYM); SYMADDR(e_, END); \
    unsigned n_ = (e_ > a_) ? (unsigned)(e_ - a_) - 4u : 0u; unsigned o_ = (unsigned)(CHUNK) * 8192u + (unsigned)(LANE) * 128u; \
    o_ = o_ < n_ ? o_ : n_; \
    asm volatile("global_load_dword %0, %1, %2" : "=v"(DST) : "v"(o_), "s"(a_) : "memory"); } while (0)
DI void keep(unsigned v) { asm volatile("" ::"v"(v)); }

#define STAMPV(i)
#define STAMPDECL
#define PS(i)
#define STAMPARG
#define STAMPPASS
#define STAMPDRAIN
#define STAMPOUT(K, N, B)

constexpr int NTILE = 288, NSTAT = 64, NMOD = 54;
constexpr int TRS = 160;
extern "C" __global__ __launch_bounds__(256)
void k_pre(const u16* __restrict__ Aq, const u16* __restrict__ Ak, const u16* __restrict__ Av,
           const u16* __restrict__ shk, const u16* __restrict__ cvec, const u16* __restrict__ wmod,
           char* __restrict__ ws) {
  __shared__ __attribute__((aligned(16))) char smem[2 * 64 * TRS];
  const int tid = threadIdx.x, lane = tid & 63, wid = tid >> 6;
  const int bid = blockIdx.x;
  unsigned pf;
  CODEPF(pf, "k_pre", "k_gemm1", wid, lane);
  Aq = gpin(Aq); Ak = gpin(Ak); Av = gpin(Av); shk = gpin(shk); cvec = gpin(cvec); wmod = gpin(wmod); ws = gpin(ws);
  if (bid < NMOD) {
    const int nb = bid;
    const int cg = tid & 7, rg = tid >> 3;
    float a[8];
#pragma unroll
    for (int j = 0; j < 8; ++j) a[j] = 0.f;
    u32x4 wv[18];
#pragma unroll
    for (int i = 0; i < 18; ++i) wv[i] = *(const u32x4*)(wmod + (size_t)(rg + 32 * i) * 3456 + nb * 64 + cg * 8);
    u16 cr[18];
#pragma unroll
    for (int i = 0; i < 18; ++i) cr[i] = cvec[rg + 32 * i];
    SCHED0;
#pragma unroll
    for (int i = 0; i < 18; ++i) {
      float cv = bf2f(cr[i]);
      float sv = rbf(cv / (1.f + __expf(-cv)));
#pragma unroll
      for (int p = 0; p < 4; ++p) { a[2 * p] += sv * bflo(wv[i][p]); a[2 * p + 1] += sv * bfhi(wv[i][p]); }
    }
#pragma unroll
    for (int j = 0; j < 8; ++j) {
      a[j] += __shfl_xor(a[j], 8); a[j] += __shfl_xor(a[j], 16); a[j] += __shfl_xor(a[j], 32);
    }
    float* red = (float*)smem;
    if (lane < 8) {
#pragma unroll
      for (int j = 0; j < 8; ++j) red[wid * 64 + lane * 8 + j] = a[j];
    }
    __syncthreads();
    if (tid < 64) {
      float r = (red[tid] + red[64 + tid]) + (red[128 + tid] + red[192 + tid]);
      r = rbf(r);
      const int n = nb * 64 + tid;
      ((float*)(ws + WS_MOD))[n] = r;
      if (n >= 3 * D_ && n < 5 * D_) ((u16*)(ws + WS_MODB))[n - 3 * D_] = (u16)pack2(n < 4 * D_ ? r : 1.f + r, 0.f);
    }
  } else if (bid < NMOD + NTILE) {
    STAMPDECL STAMPV(0);
    const int tb = bid - NMOD;
    const int st = tb / 9, dt = tb % 9;
    const int r2 = tid >> 3, c8 = tid & 7;
    const int sA = st * 64 + 2 * r2;
    const int ch = dt * 64 + c8 * 8;
    u32x4 kr[5], vr[2], qr[2], wr[4];
#pragma unroll
    for (int i = 0; i < 5; ++i) {
      int s = sA - 3 + i;
      kr[i] = *(const u32x4*)(Ak + (size_t)(s < 0 ? 0 : s) * D_ + ch);
    }
#pragma unroll
    for (int i = 0; i < 2; ++i) {
      vr[i] = *(const u32x4*)(Av + (size_t)(sA + i) * D_ + ch);
      qr[i] = *(const u32x4*)(Aq + (size_t)(sA + i) * D_ + ch);
    }
#pragma unroll
    for (int i = 0; i < 4; ++i) wr[i] = *(const u32x4*)(shk + ch * 4 + i * 8);
    SCHED0;
#pragma unroll
    for (int i = 0; i < 3; ++i)
      if (sA - 3 + i < 0) kr[i] = (u32x4){0u, 0u, 0u, 0u};
    char* kvS = smem;
    char* qS = smem + 64 * TRS;
#pragma unroll
    for (int i = 0; i < 2; ++i) {
      u32x4 o;
#pragma unroll
      for (int p = 0; p < 4; ++p) {
        float a0 = bflo(kr[i][p]) * bfhi(wr[p][1]);
        a0 = fmaf(bflo(kr[i + 1][p]), bflo(wr[p][1]), a0);
        a0 = fmaf(bflo(kr[i + 2][p]), bfhi(wr[p][0]), a0);
        a0 = fmaf(bflo(kr[i + 3][p]), bflo(wr[p][0]), a0);
        float a1 = bfhi(kr[i][p]) * bfhi(wr[p][3]);
        a1 = fmaf(bfhi(kr[i + 1][p]), bflo(wr[p][3]), a1);
        a1 = fmaf(bfhi(kr[i + 2][p]), bfhi(wr[p][2]), a1);
        a1 = fmaf(bfhi(kr[i + 3][p]), bflo(wr[p][2]), a1);
        unsigned ks = pack2(a0, a1);
        o[p] = pack2(bflo(ks) * bflo(vr[i][p]), bfhi(ks) * bfhi(vr[i][p]));
      }
      *(u32x4*)(kvS + (63 - (2 * r2 + i)) * TRS + c8 * 16) = o;
      *(u32x4*)(qS + (2 * r2 + i) * TRS + c8 * 16) = qr[i];
    }
    __syncthreads();
    STAMPV(1);
    {
      const int g = lane >> 4, q = (lane >> 2) & 3, p = lane & 3;
      const unsigned base = (unsigned)(size_t)(lds_cp)smem + (8 * g + q) * TRS + wid * 32 + p * 8;
      const int d = dt * 64 + wid * 16 + (lane & 15);
      u16* kvo = (u16*)(ws + WS_KVR) + (size_t)d * S_ + (1984 - 64 * st) + 8 * g;
      u16* qo = (u16*)(ws + WS_QT) + (size_t)d * S_ + 64 * st + 8 * g;
#pragma unroll
      for (int hf = 0; hf < 2; ++hf) {
        u32x2 a = trread(base + hf * 32 * TRS), b = trread(base + (hf * 32 + 4) * TRS);
        u32x2 c = trread(base + 64 * TRS + hf * 32 * TRS), e = trread(base + 64 * TRS + (hf * 32 + 4) * TRS);
        *(u32x4*)(kvo + hf * 32) = (u32x4){a[0], a[1], b[0], b[1]};
        *(u32x4*)(qo + hf * 32) = (u32x4){c[0], c[1], e[0], e[1]};
      }
    }
    STAMPV(2); STAMPDRAIN; STAMPV(3); STAMPOUT(3, 4, tb);
  } else {
    const int sb = bid - NMOD - NTILE;
    const int row = sb * 32 + wid * 8 + (lane >> 3), c = lane & 7;
    u32x4 v[9];
#pragma unroll
    for (int i = 0; i < 9; ++i) v[i] = *(const u32x4*)(Aq + (size_t)row * D_ + (c + 8 * i) * 8);
    if (tid < 64) *(u32x4*)(ws + WS_FLAG + sb * 1024 + tid * 16) = (u32x4){0u, 0u, 0u, 0u};
    else if (tid < 80) *(u32x4*)(ws + WS_ST2 + sb * 256 + (tid - 64) * 16) = (u32x4){0u, 0u, 0u, 0u};
    float s1 = 0.f;
#pragma unroll
    for (int i = 0; i < 9; ++i)
#pragma unroll
      for (int p = 0; p < 4; ++p) s1 = dot2(v[i][p], 0x3f803f80u, s1);
    s1 += __shfl_xor(s1, 1); s1 += __shfl_xor(s1, 2); s1 += __shfl_xor(s1, 4);
    const float mean = s1 * (1.f / D_);
    float s2 = 0.f;
#pragma unroll
    for (int i = 0; i < 9; ++i)
#pragma unroll
      for (int p = 0; p < 4; ++p) {
        float a = bflo(v[i][p]) - mean, b = bfhi(v[i][p]) - mean;
        s2 = fmaf(a, a, s2); s2 = fmaf(b, b, s2);
      }
    s2 += __shfl_xor(s2, 1); s2 += __shfl_xor(s2, 2); s2 += __shfl_xor(s2, 4);
    if (c == 0) {
      float sd = rbf(sqrtf(rbf(rbf(s2 * (1.f / D_)) + 1e-5f)));
      *(f32x2*)(ws + WS_ST1 + row * 8) = (f32x2){rbf(mean), 1.f / sd};
    }
  }
  keep(pf);
}

constexpr int NCONV = 144, NUV = 72, NWARM = 36;
constexpr int KCS = 4160;
constexpr int KER_BYTES = 4 * KCS;
constexpr int RS = 4608;
constexpr int YS = 4096 + 64;
constexpr int CB_R = 4 * KER_BYTES;
constexpr int CB_Q = CB_R + 4 * RS;
constexpr int CONV_LDS = CB_Q + 4 * YS;
constexpr int CB_X = 0;
constexpr int CB_Y = 32768;

constexpr int tri_start(int u) { return 8 * u - u * (u - 1) / 2; }
constexpr int tri_row(int n) { int u = 0; while (tri_start(u + 1) <= n) ++u; return u; }
extern "C" __global__ __launch_bounds__(512, 1)
void k_conv(const u16* __restrict__ ssmk, const u16* __restrict__ ssmD, const u16* __restrict__ Wg,
            const u16* __restrict__ Wu, const u16* __restrict__ Wd, char* __restrict__ ws) {
  extern __shared__ __attribute__((aligned(16))) char smem[];
  const int tid = threadIdx.x, lane = tid & 63;
  const int wid = __builtin_amdgcn_readfirstlane(tid >> 6);
  const int bid = blockIdx.x;
  unsigned pf;
  CODEPF(pf, "k_conv", "k_gemm2", wid, lane);
  ssmk = gpin(ssmk); ssmD = gpin(ssmD); Wg = gpin(Wg); Wu = gpin(Wu); Wd = gpin(Wd); ws = gpin(ws);
  const float* mod = (const float*)(ws + WS_MOD);
  if (bid < NCONV) {
    STAMPDECL STAMPV(0);
    const unsigned lds0 = (unsigned)(size_t)(lds_cp)smem;
    const int grp = (bid & 7) * 18 + (bid >> 3);
    const int c0 = grp * 4;
    const int cw = wid & 3, hw = wid >> 2;
    f32x2 st1[4];
    float Dv;
    {
      const u16* kerg = ssmk + (size_t)(c0 + cw) * S_;
      char* kb = smem + cw * KER_BYTES;
      u32x4 a[2]; u32x2 b[2];
#pragma unroll
      for (int i = 0; i < 2; ++i) {
        int ch = (hw * 2 + i) * 64 + lane;
        a[i] = *(const u32x4*)(kerg + ch * 8);
        b[i] = *(const u32x2*)(kerg + (ch < 255 ? ch * 8 + 8 : 0));
      }
      SCHED0;
      {
        const char* kvsrc = ws + WS_KVR + (size_t)(c0 + cw) * 4096 + hw * 2048;
        const char* qsrc = ws + WS_QT + (size_t)(c0 + cw) * 4096 + hw * 2048;
        const unsigned vo = lane * 16;
        dma1k<0>(kvsrc, vo, lds0 + CB_R + cw * RS + hw * 2048);
        dma1k<1024>(kvsrc, vo + 1024, lds0 + CB_R + cw * RS + hw * 2048);
        dma1k<0>(qsrc, vo, lds0 + CB_Q + cw * YS + hw * 2048);
        dma1k<1024>(qsrc, vo + 1024, lds0 + CB_Q + cw * YS + hw * 2048);
      }
#pragma unroll
      for (int i = 0; i < 4; ++i) st1[i] = *(const f32x2*)(ws + WS_ST1 + (size_t)(512 * i + tid) * 8);
      Dv = bf2f(ssmD[c0 + cw]);
      SCHED0;
      if (hw == 1 && lane == 63) b[1] = (u32x2){0u, 0u};
#pragma unroll
      for (int i = 0; i < 2; ++i) {
        int ch = (hw * 2 + i) * 64 + lane;
        u32x4 c1, c2, c3;
        c1[0] = __builtin_amdgcn_alignbit(a[i][1], a[i][0], 16); c1[1] = __builtin_amdgcn_alignbit(a[i][2], a[i][1], 16);
        c1[2] = __builtin_amdgcn_alignbit(a[i][3], a[i][2], 16); c1[3] = __builtin_amdgcn_alignbit(b[i][0], a[i][3], 16);
        c2[0] = a[i][1]; c2[1] = a[i][2]; c2[2] = a[i][3]; c2[3] = b[i][0];
        c3[0] = c1[1]; c3[1] = c1[2]; c3[2] = c1[3]; c3[3] = __builtin_amdgcn_alignbit(b[i][1], b[i][0], 16);
        int off = (ch * 8 + 16) * 2;
        *(u32x4*)(kb + off) = a[i];
        *(u32x4*)(kb + KCS + off) = c1;
        *(u32x4*)(kb + 2 * KCS + off) = c2;
        *(u32x4*)(kb + 3 * KCS + off) = c3;
        if (ch == 0) {
          u32x4 z = {0u, 0u, 0u, 0u};
          *(u32x4*)(kb) = z; *(u32x4*)(kb + KCS) = z; *(u32x4*)(kb + 2 * KCS) = z; *(u32x4*)(kb + 3 * KCS) = z;
          u32x4 e1 = z, e2 = z, e3 = z;
          e1[3] = a[i][0] << 16;
          e2[3] = a[i][0];
          e3[2] = a[i][0] << 16; e3[3] = c1[0];
          *(u32x4*)(kb + 16) = z;
          *(u32x4*)(kb + KCS + 16) = e1;
          *(u32x4*)(kb + 2 * KCS + 16) = e2;
          *(u32x4*)(kb + 3 * KCS + 16) = e3;
        }
      }
      if (hw == 1 && lane < 32) *(u32x4*)(smem + CB_R + cw * RS + 4096 + lane * 16) = (u32x4){0u, 0u, 0u, 0u};
    }
    wait_vm<0>();
    __syncthreads();
    STAMPV(1);
    f32x4 acc[8];
#pragma unroll
    for (int t = 0; t < 8; ++t) acc[t] = (f32x4){0.f, 0.f, 0.f, 0.f};
    const int b = lane & 15, g = lane >> 4;
    {
      const int x0 = 1 + b + 8 * g;
      const char* abase = smem + cw * KER_BYTES + (x0 & 3) * KCS + (x0 & ~3) * 2 + hw * 256;
      const char* gbase = smem + CB_R + cw * RS + (2032 - 16 * b + 8 * g) * 2 + hw * 256;
      bf16x8 G[2][8], A[2][8];
      unsigned a_lo = (unsigned)(size_t)(lds_cp)abase, a_hi = a_lo + 8;
      asm volatile("" : "+v"(a_lo), "+v"(a_hi));
      auto ldA = [&](int u0, int u1) {
        typedef __attribute__((address_space(3))) const u32x2* lp;
        u32x2 lo = *(lp)(size_t)(a_lo + u0 * 64 + u1 * 512);
        SCHED0;
        u32x2 hi = *(lp)(size_t)(a_hi + u0 * 64 + u1 * 512);
        SCHED0;
        return __builtin_bit_cast(bf16x8, (u32x4){lo[0], lo[1], hi[0], hi[1]});
      };
      A[0][0] = ldA(0, 0);
#pragma unroll
      for (int i = 0; i < 8; ++i) G[0][i] = *(const bf16x8*)(gbase - i * 512);
#pragma unroll
      for (int i = 1; i < 8; ++i) A[0][i] = ldA(0, i);
      SCHED0;
      static_for<0, 4>([&](auto uc) {
        constexpr int u0 = decltype(uc)::value, cur = u0 & 1, nxt = cur ^ 1;
        static_for<0, 36>([&](auto nc) {
          constexpr int n = decltype(nc)::value;
          constexpr int u1 = tri_row(n), t = u1 + (n - tri_start(u1));
          acc[t] = __builtin_amdgcn_mfma_f32_16x16x32_bf16(A[cur][u1], G[cur][t - u1], acc[t], 0, 0, 0);
          if constexpr (u0 < 3 && (n & 1) == 0 && n < 32) {
            constexpr int j = n >> 1;
            if constexpr (j == 0) A[nxt][0] = ldA(u0 + 1, 0);
            else if constexpr (j <= 8) G[nxt][j - 1] = *(const bf16x8*)(gbase + (u0 + 1) * 64 - (j - 1) * 512);
            else A[nxt][j - 8] = ldA(u0 + 1, j - 8);
          }
          SCHED0;
        });
      });
    }
    __syncthreads();
    STAMPV(2);
    {
      char* xb = smem + CB_X + wid * 4096 + lane * 16;
      if (hw == 0) {
#pragma unroll
        for (int t = 0; t < 4; ++t) *(f32x4*)(xb + t * 1024) = acc[4 + t];
      } else {
#pragma unroll
        for (int t = 0; t < 4; ++t) { *(f32x4*)(xb + t * 1024) = acc[t]; acc[t] = acc[4 + t]; }
      }
    }
    __syncthreads();
    {
      const char* xb = smem + CB_X + (wid ^ 4) * 4096 + lane * 16;
#pragma unroll
      for (int t = 0; t < 4; ++t) {
        f32x4 o = *(const f32x4*)(xb + t * 1024);
        f32x4 y = acc[t] + o;
        int s = 256 * (hw * 4 + t) + 16 * b + 4 * g;
        u32x2 kv = *(const u32x2*)(smem + CB_R + cw * RS + (2044 - s) * 2);
        unsigned p01 = pack2(y[0], y[1]), p23 = pack2(y[2], y[3]);
        unsigned d01 = pack2(bfhi(kv[1]) * Dv, bflo(kv[1]) * Dv), d23 = pack2(bfhi(kv[0]) * Dv, bflo(kv[0]) * Dv);
        u32x2 o2 = {pack2(bflo(p01) + bflo(d01), bfhi(p01) + bfhi(d01)), pack2(bflo(p23) + bflo(d23), bfhi(p23) + bfhi(d23))};
        *(u32x2*)(smem + CB_Y + cw * YS + s * 2) = o2;
      }
    }
    __syncthreads();
    STAMPV(3);
    {
      float ca1[4], sa[4], ga[4], cb1[4];
#pragma unroll
      for (int c = 0; c < 4; ++c) {
        sa[c] = mod[c0 + c]; ca1[c] = rbf(1.f + mod[D_ + c0 + c]); ga[c] = mod[2 * D_ + c0 + c];
        cb1[c] = rbf(1.f + mod[4 * D_ + c0 + c]);
      }
      const int q = (lane >> 2) & 3, p = lane & 3;
      const unsigned toff = q * YS + (wid * 64 + 16 * g + 4 * p) * 2;
      const size_t fo = (size_t)(c0 >> 4) * 1024 + ((c0 >> 3) & 1) * 512 + ((c0 >> 2) & 1) * 8;
#pragma unroll
      for (int i = 0; i < 4; ++i) {
        u32x2 y4 = trread(lds0 + CB_Y + toff + i * 1024);
        u32x2 q4 = trread(lds0 + CB_Q + toff + i * 1024);
        float x1[4], z[4];
#pragma unroll
        for (int c = 0; c < 4; ++c) {
          float x = (c & 1) ? bfhi(q4[c >> 1]) : bflo(q4[c >> 1]);
          float y = (c & 1) ? bfhi(y4[c >> 1]) : bflo(y4[c >> 1]);
          float n = (x - st1[i][0]) * st1[i][1];
          float h = rbf(fmaf(n, ca1[c], sa[c]));
          x1[c] = rbf(fmaf(ga[c], h * y, x));
          z[c] = x1[c] * cb1[c];
        }
        const int m = 512 * i + tid;
        const size_t o = (size_t)(m >> 5) * 36 * 1024 + (m & 31) * 16 + fo;
        *(u32x2*)(ws + WS_XF + o) = (u32x2){pack2(z[0], z[1]), pack2(z[2], z[3])};
        *(u32x2*)(ws + WS_X1F + o) = (u32x2){pack2(x1[0], x1[1]), pack2(x1[2], x1[3])};
      }
    }
    STAMPV(4); STAMPDRAIN; STAMPV(5); STAMPOUT(2, 6, bid);
  } else if (bid >= NCONV + NUV) {
    const char* p = (const char*)Wd + (size_t)(bid - NCONV - NUV) * (F_ * D_ * 2 / NWARM) + tid * 16;
    u32x4 t[18];
#pragma unroll
    for (int i = 0; i < 18; ++i) t[i] = *(const u32x4*)(p + i * 8192);
#pragma unroll
    for (int i = 0; i < 18; ++i) asm volatile("" ::"v"(t[i]));
  } else {
    STAMPDECL STAMPV(0);
    const int ub = bid - NCONV;
    const int mat = ub / 36, col0 = (ub % 36) * 128;
    const u16* W = mat ? Wu : Wg;
    const int cg = tid & 15, rg = tid >> 4;
    u32x4 wv[18];
#pragma unroll
    for (int i = 0; i < 18; ++i) wv[i] = *(const u32x4*)(W + (size_t)(2 * rg + (i & 1) + 64 * (i >> 1)) * F_ + col0 + cg * 8);
    unsigned c1[9], sbv[9];
#pragma unroll
    for (int i = 0; i < 9; ++i) {
      sbv[i] = *(const unsigned*)(ws + WS_MODB + (2 * rg + 64 * i) * 2);
      c1[i] = *(const unsigned*)(ws + WS_MODB + (D_ + 2 * rg + 64 * i) * 2);
    }
    SCHED0;
    float a1[8], a2[8];
#pragma unroll
    for (int j = 0; j < 8; ++j) { a1[j] = 0.f; a2[j] = 0.f; }
#pragma unroll
    for (int i = 0; i < 9; ++i) {
#pragma unroll
      for (int p = 0; p < 4; ++p) {
        unsigned lo = __builtin_amdgcn_perm(wv[2 * i + 1][p], wv[2 * i][p], 0x05040100u);
        unsigned hi = __builtin_amdgcn_perm(wv[2 * i + 1][p], wv[2 * i][p], 0x07060302u);
        a1[2 * p] = dot2(lo, c1[i], a1[2 * p]); a1[2 * p + 1] = dot2(hi, c1[i], a1[2 * p + 1]);
        a2[2 * p] = dot2(lo, sbv[i], a2[2 * p]); a2[2 * p + 1] = dot2(hi, sbv[i], a2[2 * p + 1]);
      }
    }
#pragma unroll
    for (int j = 0; j < 8; ++j) {
      a1[j] += __shfl_xor(a1[j], 16); a1[j] += __shfl_xor(a1[j], 32);
      a2[j] += __shfl_xor(a2[j], 16); a2[j] += __shfl_xor(a2[j], 32);
    }
    float* red = (float*)smem;
    if (lane < 16) {
#pragma unroll
      for (int j = 0; j < 8; ++j) { red[wid * 256 + lane * 8 + j] = a1[j]; red[wid * 256 + 128 + lane * 8 + j] = a2[j]; }
    }
    __syncthreads();
    if (tid < 256) {
      const int col = tid & 127, h = tid >> 7;
      u32x4 o = {0u, 0u, 0u, 0u};
      if (h == 0) {
        float u1 = 0.f, u2 = 0.f;
#pragma unroll
        for (int w = 0; w < 8; ++w) { u1 += red[w * 256 + col]; u2 += red[w * 256 + 128 + col]; }
        float u1h = rbf(u1), u2h = rbf(u2);
        o[0] = pack2(u1h, u1 - u1h); o[1] = pack2(u2h, u2 - u2h);
      }
      const int f = col0 + col;
      *(u32x4*)(ws + WS_UF + (size_t)(f >> 4) * 1024 + (h * 32 + mat * 16 + (f & 15)) * 16) = o;
    }
    STAMPV(1); STAMPOUT(4, 2, ub);
  }
  keep(pf);
}

template <int OFF> DI void gload16(bf16x8& dst, const char* sbase, unsigned voff) {
  asm volatile("global_load_dwordx4 %0, %1, %2 offset:%3" : "=v"(dst) : "v"(voff), "s"(sbase), "i"(OFF) : "memory");
}
template <int OFF> DI void gload16a(bf16x8& dst, const char* sbase, unsigned voff) {
  asm volatile("global_load_dwordx4 %0, %1, %2 offset:%3" : "=a"(dst) : "v"(voff), "s"(sbase), "i"(OFF) : "memory");
}
template <int OFF> DI void gload8_sc1(u32x2& dst, const char* sbase, unsigned voff) {
  asm volatile("global_load_dwordx2 %0, %1, %2 offset:%3 sc1" : "=v"(dst) : "v"(voff), "s"(sbase), "i"(OFF) : "memory");
}
template <int OFF> DI void gload16_sc1(u32x4& dst, const char* sbase, unsigned voff) {
  asm volatile("global_load_dwordx4 %0, %1, %2 offset:%3 sc1" : "=v"(dst) : "v"(voff), "s"(sbase), "i"(OFF) : "memory");
}
template <int OFF> DI void gstore16_sc1(char* sbase, unsigned voff, const u32x4& v) {
  asm volatile("global_store_dwordx4 %0, %1, %2 offset:%3 sc1\n\ts_nop 1" ::"v"(voff), "v"(v), "s"(sbase), "i"(OFF) : "memory");
}
DI void gstore8_sc1(char* sbase, unsigned voff, const u32x2& v) {
  asm volatile("global_store_dwordx2 %0, %1, %2 sc1" ::"v"(voff), "v"(v), "s"(sbase) : "memory");
}
DI void pin4(f32x4& v) { asm volatile("" : "+v"(v)); }
DI void pin4u(u32x4& v) { asm volatile("" : "+v"(v)); }
DI void pin2u(u32x2& v) { asm volatile("" : "+v"(v)); }
DI void pinb(bf16x8& v) { asm volatile("" : "+v"(v)); }
DI void pinba(bf16x8& v) { asm volatile("" : "+a"(v)); }
DI void gstore8(char* sbase, unsigned voff, const u32x2& v) {
  asm volatile("global_store_dwordx2 %0, %1, %2" ::"v"(voff), "v"(v), "s"(sbase) : "memory");
}
DI bf16x8 trfrag(unsigned addr_lo, unsigned addr_hi) {
  s16x4 lo = __builtin_amdgcn_ds_read_tr16_b64_v4i16((lds_s16x4p)(size_t)addr_lo);
  s16x4 hi = __builtin_amdgcn_ds_read_tr16_b64_v4i16((lds_s16x4p)(size_t)addr_hi);
  return __builtin_shufflevector(lo, hi, 0, 1, 2, 3, 4, 5, 6, 7);
}
template <int N> DI void wait_lgkm() { __builtin_amdgcn_s_waitcnt(0xC07F | (N << 8)); }
DI s16x4 trhalf(unsigned addr) { return __builtin_amdgcn_ds_read_tr16_b64_v4i16((lds_s16x4p)(size_t)addr); }
DI bf16x8 join(s16x4 lo, s16x4 hi) { return __builtin_shufflevector(lo, hi, 0, 1, 2, 3, 4, 5, 6, 7); }
template <int P> DI void dma_piece(const char* wsrc, const unsigned (&voff)[9], unsigned wdst) {
  dma1k<P * 1024>(wsrc, voff[P], wdst);
}
#define DMA9(SRC, DST) do { dma_piece<0>(SRC, voff, DST); dma_piece<1>(SRC, voff, DST); dma_piece<2>(SRC, voff, DST); \
    dma_piece<3>(SRC, voff, DST); dma_piece<4>(SRC, voff, DST); dma_piece<5>(SRC, voff, DST); \
    dma_piece<6>(SRC, voff, DST); dma_piece<7>(SRC, voff, DST); dma_piece<8>(SRC, voff, DST); } while (0)

constexpr int opc(int k, int t, int i) { return k * 10000 + t * 100 + i; }
constexpr int op_n(int op) { int k = op / 10000; return k == 0 ? 0 : (k == 2 || k == 6) ? 2 : k == 3 ? 3 : 1; }
constexpr int w3222(int t, int i, int f) {
  int first = (i == 0) ? 0 : 2 * i + 1, cnt = (i == 0) ? 3 : 2;
  return (f % 3 == 1 && f / 3 < cnt) ? opc(1, t, first + f / 3) : 0;
}
template <typename P> constexpr int since(int op, int s, int i) {
  int n = -1;
  for (int a = -1; a <= s; ++a)
    for (int b = 0; b < 4; ++b) {
      if (a == s && b == i) return n;
      for (int c = 0; c < 12; ++c) {
        int o = P::plan(a, b, c);
        if (n >= 0) n += op_n(o);
        if (o == op) n = 0;
      }
    }
  return n;
}
constexpr int cmin(int a, int b) { return a < b ? a : b; }

constexpr int G1_UP = 18432 + 128;
constexpr int SLOT1 = 37120;
constexpr int G1_UF = 4 * SLOT1;
constexpr int GEMM1_LDS = G1_UF + 9216;
#define NA 5
struct P1 {
  static constexpr int plan(int s, int i, int c) {
    if (s == -1) return i == 0 ? (c < 9 ? opc(1, 0, c) : 0) : i == 1 ? (c < 4 ? opc(2, 0, c) : c == 4 ? opc(7, 0, 0) : 0) : 0;
    if (s == 0) {
      if (c == 9) return opc(2, 1, i);
      if (c == 10) return i == 0 ? opc(3, 0, 0) : 0;
      if (c == 11) return 0;
      if (i == 0) return opc(1, 1, c);
      if (i == 1) return (c & 1) ? 0 : opc(1, 2, c >> 1);
      if (i == 2) return (c & 1) ? opc(1, 2, 5 + (c >> 1)) : 0;
      return c < 3 ? opc(4, 0, c) : c == 8 ? opc(5, 0, 0) : 0;
    }
    if (s == 1) {
      if (c == 9) return opc(2, 2, i);
      if (c > 9) return 0;
      if (i & 1) return (c & 1) ? opc(1, 3 + (i >> 1), 5 + (c >> 1)) : 0;
      return (c & 1) ? 0 : opc(1, 3 + (i >> 1), c >> 1);
    }
    if (s <= 5) return c == 9 ? opc(2, s + 1, i) : c < 9 ? w3222(s + 3, i, c) : 0;
    if (s == 6) return i == 0 ? (c < 8 ? opc(2, 7 + (c >> 2), c & 3) : c == 8 ? opc(6, 0, 0) : c == 9 ? opc(7, 0, 1) : 0) : 0;
    return 0;
  }
  static constexpr int wait1(int s, int i) {
    int w = since<P1>(opc(2, s, i), s, i);
    if (i == 3) w = cmin(w, since<P1>(opc(1, s + 1, 8), s, i));
    if (i == 3 && s == 0) w = cmin(w, since<P1>(opc(3, 0, 0), s, i));
    if (i == 3 && s == 6) w = cmin(w, since<P1>(opc(1, 8, 8), s, i));
    return w;
  }
  static constexpr int wait(int s, int i) {
    if (s != 2) return wait1(s, i);
    int w = 63;
    for (int a = 2; a <= 5; ++a) w = cmin(w, wait1(a, i));
    return w;
  }
};
struct G1State {
  f32x16 acc[2][9];
  bf16x8 xr[4][2];
  bf16x8 wf[2][9];
  bf16x8 xt[8][2];
  bf16x8 sv[3];
  u32x2 sg[2];
  float s1, s2;
  unsigned pf[2];
};
struct G1Ptr {
  const char *wbase, *xbase, *sx, *ufsrc, *sgsrc;
  char* stdst;
  unsigned voff[9];
  unsigned xoff0, xoff1, so, wdst0, lds0, sgoff;
  int wid, lane;
};
template <int F, int Z> DI void g1_mfma(f32x16& c, const bf16x8& a, const bf16x8& b) {
  if constexpr (Z) {
    if constexpr (F < NA) asm volatile("v_mfma_f32_32x32x16_bf16 %0, %1, %2, 0" : "=a"(c) : "v"(a), "v"(b));
    else asm volatile("v_mfma_f32_32x32x16_bf16 %0, %1, %2, 0" : "=&v"(c) : "v"(a), "v"(b));
  } else {
    if constexpr (F < NA) asm volatile("v_mfma_f32_32x32x16_bf16 %0, %1, %2, %0" : "+a"(c) : "v"(a), "v"(b));
    else asm volatile("v_mfma_f32_32x32x16_bf16 %0, %1, %2, %0" : "+v"(c) : "v"(a), "v"(b));
  }
}
template <int F> DI void g1_mfma_ba(f32x16& c, const bf16x8& a, const bf16x8& b) {
  if constexpr (F < NA) asm volatile("v_mfma_f32_32x32x16_bf16 %0, %1, %2, %0" : "+a"(c) : "v"(a), "a"(b));
  else asm volatile("v_mfma_f32_32x32x16_bf16 %0, %1, %2, %0" : "+v"(c) : "v"(a), "a"(b));
}
template <int F> DI void g1_pin(f32x16& c0, f32x16& c1) {
  if constexpr (F < NA) asm volatile("" : "+a"(c0), "+a"(c1));
  else asm volatile("" : "+v"(c0), "+v"(c1));
}
template <int F> DI float g1_get(const f32x16& c, int r) {
  if constexpr (F < NA) {
    float x;
    asm volatile("v_accvgpr_read_b32 %0, %1" : "=v"(x) : "a"(c[r]));
    return x;
  } else return c[r];
}
template <int I, int F> DI bf16x8 g1_frag(unsigned rd) {
  return trfrag(rd + (16 * I) * 288 + 32 * F, rd + (16 * I + 4) * 288 + 32 * F);
}
template <int I, int F> DI bf16x8 g1_lastfrag(unsigned rd7, unsigned rd8, unsigned ufrd) {
  if constexpr (I < 4) return g1_frag<I, F>(rd7);
  else if constexpr (I < 8) return g1_frag<I - 4, F>(rd8);
  else return *(const __attribute__((address_space(3))) bf16x8*)(size_t)(ufrd + F * 1024);
}
DI float dpp_ror_add(float v, int) { return v; }
template <int CTRL> DI float dpp_add(float v) {
  return v + __int_as_float(__builtin_amdgcn_update_dpp(0, __float_as_int(v), CTRL, 0xf, 0xf, false));
}
DI float swap16_add(float v) {
  auto r = __builtin_amdgcn_permlane16_swap(__float_as_uint(v), __float_as_uint(v), false, false);
  return __uint_as_float(r[0]) + __uint_as_float(r[1]);
}
DI float swap32_add(float v) {
  auto r = __builtin_amdgcn_permlane32_swap(__float_as_uint(v), __float_as_uint(v), false, false);
  return __uint_as_float(r[0]) + __uint_as_float(r[1]);
}
template <int C> DI void g1_stats(G1State& st, const G1Ptr& p) {
  if constexpr (C < 3) {
    constexpr int j = (C + 2) % 3;
    if constexpr (C == 0) { st.s1 = 0.f; st.s2 = 0.f; }
    u32x4 t = __builtin_bit_cast(u32x4, st.sv[j]);
#pragma unroll
    for (int q = 0; q < 4; ++q) { st.s1 = dot2(t[q], 0x3f803f80u, st.s1); st.s2 = dot2(t[q], t[q], st.s2); }
    if constexpr (C == 0) { if (p.lane >= 16) { st.s1 = 0.f; st.s2 = 0.f; } }
  } else if constexpr (C == 3) {
    st.s1 = dpp_add<0x122>(st.s1); st.s2 = dpp_add<0x122>(st.s2);
    st.s1 = dpp_add<0x124>(st.s1); st.s2 = dpp_add<0x124>(st.s2);
  } else if constexpr (C == 4) {
    st.s1 = dpp_add<0x128>(st.s1); st.s2 = dpp_add<0x128>(st.s2);
  } else if constexpr (C == 5) {
    st.s1 = swap16_add(st.s1); st.s2 = swap16_add(st.s2);
  } else if constexpr (C == 6) {
    st.s1 = swap32_add(st.s1); st.s2 = swap32_add(st.s2);
  } else if constexpr (C == 7) {
    st.s1 = st.s1 * (1.f / D_);
    st.s2 = rbf(rbf(fmaxf(st.s2 * (1.f / D_) - st.s1 * st.s1, 0.f)) + 1e-5f);
  } else {
    const float sd = __builtin_amdgcn_sqrtf(st.s2);
    u32x2 gr = {pack2(-st.s1, -st.s1), pack2(sd, sd)};
    gstore8_sc1(p.stdst, (p.lane & 1) * 8, gr);
  }
}
constexpr size_t WSTEP1 = (size_t)64 * F_ * 2;
template <int S, int OP>
DI void g1_op(G1State& st, const G1Ptr& p, const char* wsrc, unsigned wdst, const char* xsrc) {
  constexpr int k = OP / 10000, t = (OP / 100) % 100, i = OP % 100;
  if constexpr (k == 1) {
    if constexpr (S == 2) dma_piece<i>(wsrc, p.voff, wdst);
    else dma_piece<i>(p.wbase + t * WSTEP1, p.voff, p.wdst0 + (t & 3) * SLOT1);
  } else if constexpr (k == 2) {
    if constexpr (S == 6) {
      gload16a<i * 1024>(st.xt[(t - 7) * 4 + i][0], p.xbase + t * 4096, p.xoff0);
      gload16a<i * 1024>(st.xt[(t - 7) * 4 + i][1], p.xbase + t * 4096, p.xoff1);
    } else {
      gload16<i * 1024>(st.xr[i][0], xsrc, p.xoff0); gload16<i * 1024>(st.xr[i][1], xsrc, p.xoff1);
    }
  } else if constexpr (k == 3) {
    gload16<0>(st.sv[0], p.sx, p.so);
    gload16<0>(st.sv[1], p.sx, p.so + 16384);
    gload16<0>(st.sv[2], p.sx, (p.so & 4095) + 32768);
  } else if constexpr (k == 4) {
    int q = p.wid + 4 * i; q = q > 8 ? 8 : q;
    dma1k<G1_UF>(p.ufsrc + q * 1024, p.xoff0, p.lds0 + q * 1024);
  } else if constexpr (k == 6) {
    gload8_sc1<0>(st.sg[0], p.sgsrc, p.sgoff); gload8_sc1<256>(st.sg[1], p.sgsrc, p.sgoff);
  } else if constexpr (k == 7) {
    if constexpr (i == 0) CODEPF(st.pf[0], "k_gemm1", "k_gemm2", p.wid, p.lane);
    else CODEPF(st.pf[1], "k_gemm2", "k_zend", p.wid, p.lane);
  }
}
template <int S>
DI void g1_super(G1State& st, const G1Ptr& p, unsigned rd, unsigned rd_next, unsigned rd_next2, const char* wsrc,
                 unsigned wdst, const char* xsrc) {
  static_for<0, 4>([&](auto ic) {
    constexpr int i = decltype(ic)::value;
    constexpr int cur = i & 1, nxt = cur ^ 1;
    wait_vm<P1::wait(S, i)>();
    pinb(st.xr[i][0]); pinb(st.xr[i][1]);
    if constexpr (S == 0 && i == 3) { pinb(st.sv[0]); pinb(st.sv[1]); pinb(st.sv[2]); }
    if constexpr (i == 3) {
      wait_lgkm<0>();
      asm volatile("" ::: "memory");
      __builtin_amdgcn_s_barrier();
    }
    SCHED0;
    static_for<0, 9>([&](auto fc) {
      constexpr int f = decltype(fc)::value;
      if constexpr (i != 3 && f == 0) { wait_lgkm<8>(); SCHED0; }
      if constexpr (i != 3 && f == 5) { wait_lgkm<10>(); SCHED0; }
      g1_mfma<f, (S == 0 && i == 0)>(st.acc[0][f], st.wf[cur][f], st.xr[i][0]);
      if constexpr (i < 3) st.wf[nxt][f] = g1_frag<i + 1, f>(rd);
      else if constexpr (S != 6) st.wf[nxt][f] = g1_frag<0, f>(rd_next);
      else st.wf[nxt][f] = g1_lastfrag<f, 0>(rd_next, rd_next2, p.lds0 + G1_UF + p.xoff0);
      SCHED0;
      g1_mfma<f, (S == 0 && i == 0)>(st.acc[1][f], st.wf[cur][f], st.xr[i][1]);
      g1_op<S, P1::plan(S, i, f)>(st, p, wsrc, wdst, xsrc);
      if constexpr (S == 0 && i == 3) g1_stats<f>(st, p);
      SCHED0;
    });
    g1_op<S, P1::plan(S, i, 9)>(st, p, wsrc, wdst, xsrc);
    g1_op<S, P1::plan(S, i, 10)>(st, p, wsrc, wdst, xsrc);
    SCHED0;
  });
}

#define PS1(i) STAMPV(i)
#define HST "sc1"
DI void hstore(char* sbase, unsigned voff, u32x4 v) {
  asm volatile("global_store_dwordx4 %0, %1, %2 " HST "\n\ts_nop 1" ::"v"(voff), "v"(v), "s"(sbase) : "memory");
}
template <int F> DI void g1_epa(const f32x16& c, int r, float& g0, float& g1, float& u0, float& u1) {
  if constexpr (F < NA) {
    asm volatile("v_accvgpr_read_b32 %0, %4\n\tv_accvgpr_read_b32 %1, %5\n\tv_accvgpr_read_b32 %2, %6\n\t"
                 "v_accvgpr_read_b32 %3, %7\n\tv_max_i32 %0, 0, %0"
                 : "=&v"(g0), "=&v"(g1), "=&v"(u0), "=&v"(u1) : "a"(c[r]), "a"(c[r + 1]), "a"(c[r + 8]), "a"(c[r + 9]));
  } else {
    asm volatile("v_max_i32 %0, 0, %2\n\tv_max_i32 %1, 0, %3\n\tv_mul_f32 %0, %0, %4"
                 : "=&v"(g0), "=&v"(g1) : "v"(c[r]), "v"(c[r + 1]), "v"(c[r + 8]));
    u1 = c[r + 9];
  }
}
template <int F> DI unsigned g1_epb(float g0, float g1, float u0, float u1) {
  unsigned o;
  if constexpr (F < NA) {
    asm volatile("v_max_i32 %2, 0, %2\n\tv_mul_f32 %1, %1, %3\n\tv_mul_f32 %2, %2, %4\n\tv_cvt_pk_bf16_f32 %0, %1, %2"
                 : "=v"(o), "+v"(g0), "+v"(g1) : "v"(u0), "v"(u1));
  } else {
    asm volatile("v_mul_f32 %1, %1, %3\n\tv_cvt_pk_bf16_f32 %0, %2, %1" : "=v"(o), "+v"(g1) : "v"(g0), "v"(u1));
  }
  return o;
}

extern "C" __global__ __launch_bounds__(256, 1)
void k_gemm1(const u16* __restrict__ Wg, const u16* __restrict__ Wu, char* __restrict__ ws) {
  extern __shared__ __attribute__((aligned(16))) char smem[];
  const int tid = threadIdx.x, lane = tid & 63;
  const int wid = __builtin_amdgcn_readfirstlane(tid >> 6);
  const int bid = blockIdx.x;
  const int xcd = bid & 7, idx = bid >> 3;
  const int cb = (xcd >> 1) * 8 + (idx & 7), rb = (xcd & 1) * 4 + (idx >> 3);
  STAMPDECL STAMPV(0);
  G1Ptr p;
  p.lds0 = (unsigned)(size_t)(lds_cp)smem;
  p.wid = wid; p.lane = lane;
  p.wbase = (const char*)((wid & 2) ? Wu : Wg) + (size_t)(32 * (wid & 1)) * (F_ * 2) + cb * 288;
  p.xbase = ws + WS_XF + (size_t)(rb * 8 + wid * 2) * 36 * 1024;
#pragma unroll
  for (int i = 0; i < 9; ++i) { int c = 64 * i + lane; p.voff[i] = (c / 18) * (F_ * 2) + (c % 18) * 16; }
  p.xoff0 = lane * 16; p.xoff1 = lane * 16 + 36 * 1024;
  p.wdst0 = p.lds0 + (wid >> 1) * G1_UP + (wid & 1) * 9216;
  G1State st;
  static_for<0, 9>([&](auto cc) { g1_op<-1, P1::plan(-1, 0, decltype(cc)::value)>(st, p, nullptr, 0, nullptr); });
  static_for<0, 5>([&](auto cc) { g1_op<-1, P1::plan(-1, 1, decltype(cc)::value)>(st, p, nullptr, 0, p.xbase); });
  SCHED0;
  const int srow = rb * 256 + cb * 8 + wid * 2;
  p.sx = ws + WS_X1F + (size_t)(srow >> 5) * 36 * 1024 + (srow & 31) * 16;
  p.so = (lane >> 1) * 512 + (lane & 1) * 16;
  p.stdst = ws + WS_ST2 + srow * 8;
  p.ufsrc = ws + WS_UF + (size_t)cb * 9 * 1024;
  p.sgsrc = ws + WS_ST2 + (rb * 256 + wid * 64) * 8;
  p.sgoff = (lane & 31) * 8;
  const int G = lane >> 4;
  const unsigned rd0 = p.lds0 + (G & 1) * G1_UP + (8 * (G >> 1) + ((lane >> 2) & 3)) * 288 + 8 * (lane & 3);
  unsigned rd1 = rd0 + SLOT1, rd2 = rd0 + 2 * SLOT1;
  asm volatile("" : "+v"(rd1), "+v"(rd2));
  SCHED0;
  wait_vm<since<P1>(opc(1, 0, 8), 0, 0)>();
  __builtin_amdgcn_s_barrier();
  SCHED0;
  STAMPV(1);
  static_for<0, 9>([&](auto fc) { constexpr int f = decltype(fc)::value; st.wf[0][f] = g1_frag<0, f>(rd0); });
  SCHED0;
  g1_super<0>(st, p, rd0, rd1, 0, nullptr, 0, p.xbase + 4096);
  keep(st.pf[0]);
  PS1(2);
  g1_super<1>(st, p, rd1, rd2, 0, nullptr, 0, p.xbase + 2 * 4096);
  PS1(3);
#pragma unroll 1
  for (int s = 2; s < 6; ++s) {
    g1_super<2>(st, p, rd0 + (s & 3) * SLOT1, rd0 + ((s + 1) & 3) * SLOT1, 0, p.wbase + (size_t)(s + 3) * WSTEP1,
                p.wdst0 + ((s + 3) & 3) * SLOT1, p.xbase + (s + 1) * 4096);
  }
  unsigned rd3 = rd0 + 3 * SLOT1;
  asm volatile("" : "+v"(rd3));
  g1_super<6>(st, p, rd2, rd3, rd0, nullptr, 0, nullptr);
  PS1(4);
  {
    const int h = lane >> 5, ml = lane & 31;
    char* hbase = ws + WS_HF + (size_t)((rb * 8 + wid * 2) * 2) * 144 * 1024;
    const unsigned hoff = (ml >> 4) * (144 * 1024) + (h * 16 + (ml & 15)) * 16 + cb * 9 * 512;
    const unsigned ufrd = p.lds0 + G1_UF + p.xoff0;
    wait_vm<0>();
#pragma unroll
    for (int i = 0; i < 8; ++i) { pinba(st.xt[i][0]); pinba(st.xt[i][1]); }
    pin2u(st.sg[0]); pin2u(st.sg[1]); keep(st.pf[1]);
#pragma unroll 1
    for (int spin = 0; spin < (1 << 22); ++spin) {
      if (__builtin_amdgcn_ballot_w64(st.sg[0][1] == 0u || st.sg[1][1] == 0u) == 0ull) break;
      __builtin_amdgcn_s_sleep(2);
      gload8_sc1<0>(st.sg[0], p.sgsrc, p.sgoff); gload8_sc1<256>(st.sg[1], p.sgsrc, p.sgoff);
      wait_vm<0>();
      pin2u(st.sg[0]); pin2u(st.sg[1]);
    }
    bf16x8 sb[2];
#pragma unroll
    for (int m = 0; m < 2; ++m) sb[m] = __builtin_bit_cast(bf16x8, (u32x4){st.sg[m][0], st.sg[m][1], 0u, 0u});
    SCHED0;
    static_for<0, 10>([&](auto fc) {
      constexpr int f = decltype(fc)::value;
      constexpr int cur = f & 1, nxt = cur ^ 1;
      u32x4 o;
      float g0, g1, u0, u1;
      static_for<0, 18>([&](auto nc) {
        constexpr int n = decltype(nc)::value;
        constexpr int i = n >> 1, m = n & 1;
        if constexpr (f < 9) {
          if constexpr (n == 0) { wait_lgkm<7>(); SCHED0; }
          if constexpr (n == 10) { if constexpr (f < 8) wait_lgkm<10>(); else wait_lgkm<0>(); SCHED0; }
          if constexpr (i < 8) g1_mfma_ba<f>(st.acc[m][f], st.wf[cur][i], st.xt[i][m]);
          else g1_mfma<f, 0>(st.acc[m][f], st.wf[cur][i], sb[m]);
          if constexpr (f < 8 && m == 0) st.wf[nxt][i] = g1_lastfrag<i, f + 1>(rd3, rd0, ufrd);
          SCHED0;
        } else if constexpr (n == 0) {
          asm volatile("s_nop 15\n\ts_nop 7" ::: "memory");
          SCHED0;
        }
        if constexpr (f > 0 && n >= 1 && n <= 16) {
          constexpr int e = f - 1, u = (n - 1) >> 1, em = u >> 2, r = 2 * (u & 3);
          if constexpr (n == 1) g1_pin<e>(st.acc[0][e], st.acc[1][e]);
          if constexpr (n & 1) {
            g1_epa<e>(st.acc[em][e], r, g0, g1, u0, u1);
          } else {
            o[u & 3] = g1_epb<e>(g0, g1, u0, u1);
            if constexpr ((u & 3) == 3) hstore(hbase + em * 2 * 144 * 1024, hoff + e * 512, o);
          }
          if constexpr (f < 9) SCHED0;
        }
      });
    });
  }
  PS1(5); STAMPOUT(0, 6, bid);
}

constexpr int SLOT2 = 36864;
constexpr int GEMM2_LDS = 4 * SLOT2;
struct P2 {
  static constexpr int plan(int s, int i, int c) {
    if (s == -1) return (i < 3 && c < 4) ? opc(2, i, c) : 0;
    if (s <= 5) return c == 9 ? opc(2, s + 3, i) : 0;
    return 0;
  }
  static constexpr int wait(int s, int i) { return since<P2>(opc(2, s, i), s, i); }
};
struct G2State {
  f32x4 acc[2][9];
  bf16x8 xr[12][2];
  bf16x8 wf[2][9];
};
struct G2Ptr {
  const char* xbase;
  unsigned xoff0, xoff1;
  unsigned rd[4];
};
template <int Z> DI void mfma16(f32x4& c, const bf16x8& a, const bf16x8& b) {
  if constexpr (Z) asm volatile("v_mfma_f32_16x16x32_bf16 %0, %1, %2, 0" : "=&v"(c) : "v"(a), "v"(b));
  else asm volatile("v_mfma_f32_16x16x32_bf16 %0, %1, %2, %0" : "+v"(c) : "v"(a), "v"(b));
}
template <int I, int F> DI bf16x8 g2_frag(unsigned rd) {
  return trfrag(rd + (32 * I) * 288 + 32 * F, rd + (32 * I + 8) * 288 + 32 * F);
}
constexpr size_t WSTEP2 = (size_t)128 * D_ * 2;
template <int OP> DI void g2_op(G2State& st, const G2Ptr& p) {
  constexpr int k = OP / 10000, t = (OP / 100) % 100, i = OP % 100;
  if constexpr (k == 2) {
    gload16<i * 1024>(st.xr[4 * (t % 3) + i][0], p.xbase + t * 4096, p.xoff0);
    gload16<i * 1024>(st.xr[4 * (t % 3) + i][1], p.xbase + t * 4096, p.xoff1);
  }
}
template <int I, int F, int H> DI s16x4 g2_half(unsigned rd) { return trhalf(rd + (32 * I + 8 * H) * 288 + 32 * F); }
template <int S>
DI void g2_super(G2State& st, const G2Ptr& p) {
  constexpr int R = S % 3;
  static_for<0, 4>([&](auto ic) {
    constexpr int i = decltype(ic)::value;
    constexpr int cur = i & 1, nxt = cur ^ 1;
    wait_vm<P2::wait(S, i)>();
    pinb(st.xr[4 * R + i][0]); pinb(st.xr[4 * R + i][1]);
    if constexpr (i == 3) {
      asm volatile("" ::: "memory");
      __builtin_amdgcn_s_barrier();
      asm volatile("" ::: "memory");
    }
    SCHED0;
    static_for<0, 9>([&](auto fc) {
      constexpr int f = decltype(fc)::value;
      constexpr bool ld = (i < 3) || (S < 7) || (f >= 5);
      constexpr int li = (i < 3) ? i + 1 : (S < 7) ? 0 : (f >= 5 ? f - 5 : 0), lf = (i == 3 && S == 7) ? 8 : f;
      constexpr int ls = (i < 3) ? (S & 3) : ((S + 1) & 3), ld_to = (i == 3 && S == 7) ? li : f;
      if constexpr (f == 0) { wait_lgkm<8>(); SCHED0; }
      if constexpr (f == 5) { wait_lgkm<(i == 3 && S == 7) ? 0 : 10>(); SCHED0; }
      s16x4 lo;
      mfma16<(S == 0 && i == 0)>(st.acc[0][f], st.wf[cur][f], st.xr[4 * R + i][0]);
      if constexpr (ld) lo = g2_half<li, lf, 0>(p.rd[ls]);
      SCHED0;
      mfma16<(S == 0 && i == 0)>(st.acc[1][f], st.wf[cur][f], st.xr[4 * R + i][1]);
      if constexpr (ld) st.wf[nxt][ld_to] = join(lo, g2_half<li, lf, 1>(p.rd[ls]));
      SCHED0;
    });
    g2_op<P2::plan(S, i, 9)>(st, p);
    SCHED0;
  });
}
DI u32x4 g2_pack(const f32x4& a, const f32x4& b) {
  return (u32x4){pack2(a[0], a[1]), pack2(a[2], a[3]), pack2(b[0], b[1]), pack2(b[2], b[3])};
}
DI f32x4 g2_unpack(unsigned a, unsigned b) { return (f32x4){bflo(a), bfhi(a), bflo(b), bfhi(b)}; }
DI void g2_out(f32x4 s, float is2, const u32x2& x1, const f32x4& gb, u16* out, unsigned oo) {
  s = s * is2;
  float o0 = bflo(x1[0]) + rbf(gb[0] * rbf(s[0])), o1 = bfhi(x1[0]) + rbf(gb[1] * rbf(s[1]));
  float o2 = bflo(x1[1]) + rbf(gb[2] * rbf(s[2])), o3 = bfhi(x1[1]) + rbf(gb[3] * rbf(s[3]));
  u32x2 o = {pack2(o0, o1), pack2(o2, o3)};
  gstore8((char*)out, oo, o);
}
extern "C" __global__ __launch_bounds__(512, 1)
void k_gemm2(const u16* __restrict__ Wd, char* __restrict__ ws, u16* __restrict__ out) {
  extern __shared__ __attribute__((aligned(16))) char smem[];
  const int tid = threadIdx.x, lane = tid & 63;
  const int wid8 = __builtin_amdgcn_readfirstlane(tid >> 6);
  const int wid = wid8 & 3;
  const int bid = blockIdx.x;
  const int xcd = bid & 7, idx = bid >> 3;
  const int q = xcd >> 1;
  const int rb = (xcd & 1) * 8 + (idx >> 2);
  const int db = idx & 3;
  const unsigned lds0 = (unsigned)(size_t)(lds_cp)smem;
  if (wid8 >= 4) {
    const char* wsrc = (const char*)Wd + (size_t)(q * 1152 + 32 * wid) * (D_ * 2) + db * 288;
    unsigned voff[9];
#pragma unroll
    for (int i = 0; i < 9; ++i) {
      int c = 64 * i + lane, j = c % 18;
      voff[i] = (c / 18) * (D_ * 2) + (j < 16 ? ((j + 4 * q + 4) & 15) : j) * 16;
    }
    const unsigned wdst0 = lds0 + wid * 9216;
    unsigned pf;
    DMA9(wsrc, wdst0);
    CODEPF(pf, "k_gemm2", "k_zend", wid, lane);
    DMA9(wsrc + WSTEP2, wdst0 + SLOT2);
    wait_vm<9>();
    keep(pf);
    __builtin_amdgcn_s_barrier();
    DMA9(wsrc + 2 * WSTEP2, wdst0 + 2 * SLOT2);
    DMA9(wsrc + 3 * WSTEP2, wdst0 + 3 * SLOT2);
    wait_vm<18>();
    __builtin_amdgcn_s_barrier();
#pragma unroll 1
    for (int s = 1; s < 6; ++s) {
      wait_vm<9>();
      __builtin_amdgcn_s_barrier();
      DMA9(wsrc + (s + 3) * WSTEP2, wdst0 + ((s + 3) & 3) * SLOT2);
    }
    wait_vm<9>();
    __builtin_amdgcn_s_barrier();
    wait_vm<0>();
    __builtin_amdgcn_s_barrier();
    return;
  }
  STAMPDECL STAMPV(0);
  G2Ptr p;
  p.xbase = ws + WS_HF + ((size_t)(rb * 8 + wid * 2) * 144 + q * 36) * 1024;
  p.xoff0 = lane * 16; p.xoff1 = lane * 16 + 144 * 1024;
  G2State st;
  static_for<0, 3>([&](auto ic) {
    static_for<0, 4>([&](auto cc) { g2_op<P2::plan(-1, decltype(ic)::value, decltype(cc)::value)>(st, p); });
  });
  SCHED0;
  const int g = lane >> 4, ml = lane & 15;
  const unsigned rd0 = lds0 + (16 * (g >> 1) + 4 * (g & 1) + ((lane >> 2) & 3)) * 288 + 8 * (lane & 3);
#pragma unroll
  for (int i = 0; i < 4; ++i) p.rd[i] = rd0 + i * SLOT2;
  asm volatile("" : "+v"(p.rd[0]), "+v"(p.rd[1]), "+v"(p.rd[2]), "+v"(p.rd[3]));
  const int tw = (rb * 4 + db) * 4 + wid;
  char* pb = ws + WS_PART + (size_t)tw * 9 * 4096;
  char* fb = ws + WS_FLAG + (size_t)tw * 256;
  const char* x1b = ws + WS_X1F + (size_t)(rb * 4 + wid) * 36 * 1024 + db * 9 * 1024;
  unsigned x1o = (g >> 1) * 512 + ml * 16 + (g & 1) * 8;
  unsigned gbo = (5 * D_ + db * 144 + 4 * g) * 4;
  unsigned sgo = WS_ST2 + (rb * 128 + wid * 32 + ml) * 8 + 4;
  unsigned ob = ((rb * 128 + wid * 32 + ml) * D_ + db * 144 + 4 * g) * 2;
  pb = gpin(pb); fb = gpin(fb); x1b = gpin(x1b);
  asm volatile("" : "+v"(x1o), "+v"(gbo), "+v"(sgo), "+v"(ob));
  SCHED0;
  __builtin_amdgcn_s_barrier();
  SCHED0;
  STAMPV(1);
  static_for<0, 9>([&](auto fc) { constexpr int f = decltype(fc)::value; st.wf[0][f] = g2_frag<0, f>(p.rd[0]); });
  SCHED0;
  static_for<0, 8>([&](auto sc) { g2_super<decltype(sc)::value>(st, p); });
  const unsigned lo = lane * 16;
  wait_vm<0>();
#pragma unroll
  for (int i = 0; i < 4; ++i) { pinb(st.xr[8 + i][0]); pinb(st.xr[8 + i][1]); }
  SCHED0;
  u32x2 x1[5]; f32x4 gb[3]; unsigned sg[2];
  static_for<0, 9>([&](auto nc) {
    constexpr int n = decltype(nc)::value;
    constexpr int r = (n == 0) ? 8 : n - 1, rp = (n == 1) ? 8 : n - 2;
    constexpr int cur = n & 1, nxt = cur ^ 1;
    static_for<0, 4>([&](auto ic) {
      constexpr int i = decltype(ic)::value;
      s16x4 hl;
      mfma16<0>(st.acc[0][r], st.wf[cur][i], st.xr[8 + i][0]);
      if constexpr (n < 8) hl = g2_half<i, n, 0>(p.rd[0]);
      SCHED0;
      mfma16<0>(st.acc[1][r], st.wf[cur][i], st.xr[8 + i][1]);
      if constexpr (n < 8) st.wf[nxt][i] = join(hl, g2_half<i, n, 1>(p.rd[0]));
      SCHED0;
      if constexpr (n >= 1 && n <= 7 && i == 1) {
        pin4(st.acc[0][rp]); pin4(st.acc[1][rp]);
        const int dp = (rp == 8) ? 8 : ((2 * q + 2 + rp) & 7);
        gstore16_sc1<0>(pb + dp * 4096 + q * 1024, lo, g2_pack(st.acc[0][rp], st.acc[1][rp]));
        SCHED0;
      }
      if constexpr (n == 0) {
        if constexpr (i < 2) {
          x1[2 * i] = *(const u32x2*)(x1b + (2 * q + i) * 1024 + x1o);
          x1[2 * i + 1] = *(const u32x2*)(x1b + (2 * q + i) * 1024 + 256 + x1o);
          gb[i] = *(const f32x4*)(ws + WS_MOD + (2 * q + i) * 64 + gbo);
        } else if constexpr (i == 2) {
          x1[4] = *(const u32x2*)(x1b + 8 * 1024 + (q & 1) * 256 + x1o);
          gb[2] = *(const f32x4*)(ws + WS_MOD + 8 * 64 + gbo);
        } else {
          sg[0] = *(const unsigned*)(ws + sgo);
          sg[1] = *(const unsigned*)(ws + 128 + sgo);
        }
        SCHED0;
      }
    });
  });
  STAMPV(2);
  wait_vm<0>();
  STAMPV(3);
#pragma unroll
  for (int t = 0; t < 5; ++t) pin2u(x1[t]);
#pragma unroll
  for (int t = 0; t < 3; ++t) pin4(gb[t]);
  asm volatile("" : "+v"(sg[0]), "+v"(sg[1]));
  if (lane < 4) asm volatile("global_store_dword %0, %1, %2 sc1" ::"v"(lane * 64), "v"(1), "s"(fb + q * 4) : "memory");
  float is2[2];
#pragma unroll
  for (int m = 0; m < 2; ++m) { float s = bfhi(sg[m]); is2[m] = 1.f / (s * s); }
  asm volatile("" : "+v"(is2[0]), "+v"(is2[1]));
#pragma unroll
  for (int t = 6; t < 8; ++t) { pin4(st.acc[0][t]); pin4(st.acc[1][t]); }
  SCHED0;
#pragma unroll 1
  for (int spin = 0; spin < (1 << 22); ++spin) {
    u32x4 u;
    gload16_sc1<0>(u, fb + q * 64, 0u);
    wait_vm<0>();
    pin4u(u);
    if (__builtin_amdgcn_readfirstlane(u[0] & u[1] & u[2] & u[3]) != 0) break;
    __builtin_amdgcn_s_sleep(1);
  }
  STAMPV(4);
  u32x4 pr[2][3]; u32x2 p8[3];
#pragma unroll
  for (int k = 0; k < 3; ++k) {
    const int qs = (q + 1 + k) & 3;
    gload16_sc1<0>(pr[0][k], pb + (2 * q) * 4096 + qs * 1024, lo);
    gload16_sc1<0>(pr[1][k], pb + (2 * q + 1) * 4096 + qs * 1024, lo);
    gload8_sc1<0>(p8[k], pb + 8 * 4096 + qs * 1024 + (q & 1) * 8, lo);
  }
  wait_vm<0>();
#pragma unroll
  for (int k = 0; k < 3; ++k) { pin4u(pr[0][k]); pin4u(pr[1][k]); pin2u(p8[k]); }
#pragma unroll
  for (int t = 0; t < 4; ++t) {
    const int d = t >> 1, m = t & 1;
    f32x4 v = (st.acc[m][6 + d] + g2_unpack(pr[d][0][2 * m], pr[d][0][2 * m + 1])) +
              (g2_unpack(pr[d][1][2 * m], pr[d][1][2 * m + 1]) + g2_unpack(pr[d][2][2 * m], pr[d][2][2 * m + 1]));
    g2_out(v, is2[m], x1[t], gb[d], out, ob + m * 16 * D_ * 2 + (2 * q + d) * 32);
  }
  if (q < 2) {
    f32x4 own = q ? st.acc[1][8] : st.acc[0][8];
    f32x4 v = (own + g2_unpack(p8[0][0], p8[0][1])) + (g2_unpack(p8[1][0], p8[1][1]) + g2_unpack(p8[2][0], p8[2][1]));
    g2_out(v, q ? is2[1] : is2[0], x1[4], gb[2], out, ob + q * 16 * D_ * 2 + 8 * 32);
  }
  STAMPDRAIN; STAMPV(5); STAMPOUT(1, 6, bid);
}

extern "C" __global__ void k_zend() {}

extern "C" void kernel_launch(void* const* d_in, const int* in_sizes, int n_in, void* d_out, int out_size,
                              void* d_ws, size_t ws_size, hipStream_t stream) {
  const u16* Aq = (const u16*)d_in[0];
  const u16* Ak = (const u16*)d_in[1];
  const u16* Av = (const u16*)d_in[2];
  const u16* shk = (const u16*)d_in[3];
  const u16* ssmk = (const u16*)d_in[4];
  const u16* ssmD = (const u16*)d_in[5];
  const u16* Wg = (const u16*)d_in[7];
  const u16* Wu = (const u16*)d_in[8];
  const u16* Wd = (const u16*)d_in[9];
  const u16* cvec = (const u16*)d_in[10];
  const u16* wmod = (const u16*)d_in[11];
  char* ws = (char*)d_ws;
  (void)hipFuncSetAttribute((const void*)k_conv, hipFuncAttributeMaxDynamicSharedMemorySize, CONV_LDS);
  (void)hipFuncSetAttribute((const void*)k_gemm1, hipFuncAttributeMaxDynamicSharedMemorySize, GEMM1_LDS);
  (void)hipFuncSetAttribute((const void*)k_gemm2, hipFuncAttributeMaxDynamicSharedMemorySize, GEMM2_LDS);
  k_pre<<<NMOD + NTILE + NSTAT, 256, 0, stream>>>(Aq, Ak, Av, shk, cvec, wmod, ws);
  k_conv<<<NCONV + NUV + NWARM, 512, CONV_LDS, stream>>>(ssmk, ssmD, Wg, Wu, Wd, ws);
  k_gemm1<<<256, 256, GEMM1_LDS, stream>>>(Wg, Wu, ws);
  k_gemm2<<<256, 512, GEMM2_LDS, stream>>>(Wd, ws, (u16*)d_out);
}
```

```cpp
#include <hip/hip_runtime.h>
#include <hip/hip_bf16.h>
#include <stdint.h>
#include <type_traits>

typedef __attribute__((ext_vector_type(8))) short bf16x8;
typedef __attribute__((ext_vector_type(4))) short s16x4;
typedef __attribute__((ext_vector_type(4))) float f32x4;
typedef __attribute__((ext_vector_type(2))) float f32x2;
typedef __attribute__((ext_vector_type(16))) float f32x16;
typedef __attribute__((ext_vector_type(4))) unsigned u32x4;
typedef __attribute__((ext_vector_type(2))) unsigned u32x2;
typedef __attribute__((ext_vector_type(2))) __bf16 bf16x2_t;
typedef __attribute__((address_space(3))) s16x4* lds_s16x4p;
typedef __attribute__((address_space(3))) char* lds_cp;
typedef unsigned short u16;

#define DI __device__ __forceinline__

constexpr int S_ = 2048, D_ = 576, F_ = 4608;

constexpr size_t SD2 = (size_t)S_ * D_ * 2;
constexpr size_t WS_GB   = 0;
constexpr size_t WS_ST1  = 16384;
constexpr size_t WS_ST2  = 32768;
constexpr size_t WS_FLAG = 49152;
constexpr size_t WS_MODP = WS_FLAG + 65536;
constexpr size_t WS_UF   = WS_MODP + 49152;
constexpr size_t WS_KVR  = WS_UF + 288 * 1024;
constexpr size_t WS_QG   = WS_KVR + SD2;
constexpr size_t WS_XF   = WS_QG + SD2;
constexpr size_t WS_X1F  = WS_XF + SD2;
constexpr size_t WS_HF   = WS_X1F + SD2;
constexpr size_t WS_PART = WS_HF + (size_t)S_ * F_ * 2;

DI float bf2f(unsigned u) { return __uint_as_float(u << 16); }
DI float bflo(unsigned u) { return __uint_as_float(u << 16); }
DI float bfhi(unsigned u) { return __uint_as_float(u & 0xffff0000u); }
DI unsigned pack2(float a, float b) {
  f32x2 v = {a, b};
  bf16x2_t r = __builtin_convertvector(v, bf16x2_t);
  return __builtin_bit_cast(unsigned, r);
}
DI float rbf(float a) { return bflo(pack2(a, 0.f)); }
DI float dot2(unsigned a, unsigned b, float c) {
  return __builtin_amdgcn_fdot2_f32_bf16(__builtin_bit_cast(bf16x2_t, a), __builtin_bit_cast(bf16x2_t, b), c, false);
}
DI u32x2 trread(unsigned addr) {
  return __builtin_bit_cast(u32x2, __builtin_amdgcn_ds_read_tr16_b64_v4i16((lds_s16x4p)(size_t)addr));
}
template <int I, int N, typename Fn> DI void static_for(Fn&& fn) {
  if constexpr (I < N) { fn(std::integral_constant<int, I>{}); static_for<I + 1, N>(fn); }
}
#define SCHED0 __builtin_amdgcn_sched_barrier(0)
template <typename T> DI T* gpin(T* p) {
  unsigned long long v = (unsigned long long)p;
  asm volatile("" : "+s"(v));
  return (T*)(__attribute__((address_space(1))) T*)v;
}
template <int N> DI void wait_vm() { asm volatile("s_waitcnt vmcnt(%0)" ::"i"(N) : "memory"); }
template <int OFF> DI void dma1k(const char* sbase, unsigned voff, unsigned ldsdst) {
  asm volatile("s_add_u32 m0, %2, %3\n\ts_nop 0\n\tglobal_load_lds_dwordx4 %0, %1"
               ::"v"(voff), "s"(sbase), "s"(ldsdst), "i"(OFF) : "memory", "scc");
}

#define SYMADDR(V, SYM) asm volatile("s_getpc_b64 vcc\n\ts_add_u32 vcc_lo, vcc_lo, " SYM "@rel32@lo+4\n\t" \
    "s_addc_u32 vcc_hi, vcc_hi, " SYM "@rel32@hi+12\n\ts_mov_b64 %0, vcc" : "=s"(V) :: "vcc", "scc")
#define CODEPF(DST, SYM, END, CHUNK, LANE) do { unsigned long long a_, e_; SYMADDR(a_, SYM); SYMADDR(e_, END); \
    unsigned n_ = (e_ > a_) ? (unsigned)(e_ - a_) - 4u : 0u; unsigned o_ = (unsigned)(CHUNK) * 8192u + (unsigned)(LANE) * 128u; \
    o_ = o_ < n_ ? o_ : n_; \
    asm volatile("global_load_dword %0, %1, %2" : "=v"(DST) : "v"(o_), "s"(a_) : "memory"); } while (0)
#define SCP ""
#define SCC ""
#define SCO "sc1"
DI void gstore8(char* sbase, unsigned voff, const u32x2& v) {
  asm volatile("global_store_dwordx2 %0, %1, %2 " SCO ::"v"(voff), "v"(v), "s"(sbase) : "memory");
}
DI void gstore8c(char* sbase, unsigned voff, const u32x2& v) {
  asm volatile("global_store_dwordx2 %0, %1, %2 " SCC ::"v"(voff), "v"(v), "s"(sbase) : "memory");
}
DI void st16(void* p, const u32x4& v) { asm volatile("global_store_dwordx4 %0, %1, off " SCP "\n\ts_nop 1" ::"v"(p), "v"(v) : "memory"); }
DI void st8(void* p, const u32x2& v) { asm volatile("global_store_dwordx2 %0, %1, off " SCP ::"v"(p), "v"(v) : "memory"); }
DI void st4(void* p, unsigned v) { asm volatile("global_store_dword %0, %1, off " SCP ::"v"(p), "v"(v) : "memory"); }
DI void keep(unsigned v) { asm volatile("" ::"v"(v)); }

#define STAMPV(i)
#define STAMPDECL
#define PS(i)
#define STAMPARG
#define STAMPPASS
#define STAMPDRAIN
#define STAMPOUT(K, N, B)

constexpr int NTILE = 288, NSTAT = 128, NMOD = 162;
constexpr int TRS = 160;
extern "C" __global__ __launch_bounds__(256)
void k_pre(const u16* __restrict__ Aq, const u16* __restrict__ Ak, const u16* __restrict__ Av,
           const u16* __restrict__ shk, const u16* __restrict__ cvec, const u16* __restrict__ wmod,
           char* __restrict__ ws) {
  __shared__ __attribute__((aligned(16))) char smem[64 * TRS];
  const int tid = threadIdx.x, lane = tid & 63, wid = tid >> 6;
  const int bid = blockIdx.x;
  unsigned pf;
  CODEPF(pf, "k_pre", "k_gemm1", wid, lane);
  Aq = gpin(Aq); Ak = gpin(Ak); Av = gpin(Av); shk = gpin(shk); cvec = gpin(cvec); wmod = gpin(wmod); ws = gpin(ws);
  if (bid < NTILE) {
    STAMPDECL STAMPV(0);
    const int tb = bid;
    const int st = tb / 9, dt = tb % 9;
    const int r2 = tid >> 3, c8 = tid & 7;
    const int sA = st * 64 + 2 * r2;
    const int ch = dt * 64 + c8 * 8;
    u32x4 kr[5], vr[2], qr[2], wr[4];
#pragma unroll
    for (int i = 0; i < 5; ++i) {
      int s = sA - 3 + i;
      kr[i] = *(const u32x4*)(Ak + (size_t)(s < 0 ? 0 : s) * D_ + ch);
    }
#pragma unroll
    for (int i = 0; i < 2; ++i) vr[i] = *(const u32x4*)(Av + (size_t)(sA + i) * D_ + ch);
#pragma unroll
    for (int i = 0; i < 4; ++i) wr[i] = *(const u32x4*)(shk + ch * 4 + i * 8);
#pragma unroll
    for (int i = 0; i < 2; ++i) qr[i] = *(const u32x4*)(Aq + (size_t)(sA + i) * D_ + ch);
    SCHED0;
#pragma unroll
    for (int i = 0; i < 3; ++i)
      if (sA - 3 + i < 0) kr[i] = (u32x4){0u, 0u, 0u, 0u};
#pragma unroll
    for (int i = 0; i < 2; ++i) {
      u32x4 o;
#pragma unroll
      for (int p = 0; p < 4; ++p) {
        float a0 = bflo(kr[i][p]) * bfhi(wr[p][1]);
        a0 = fmaf(bflo(kr[i + 1][p]), bflo(wr[p][1]), a0);
        a0 = fmaf(bflo(kr[i + 2][p]), bfhi(wr[p][0]), a0);
        a0 = fmaf(bflo(kr[i + 3][p]), bflo(wr[p][0]), a0);
        float a1 = bfhi(kr[i][p]) * bfhi(wr[p][3]);
        a1 = fmaf(bfhi(kr[i + 1][p]), bflo(wr[p][3]), a1);
        a1 = fmaf(bfhi(kr[i + 2][p]), bfhi(wr[p][2]), a1);
        a1 = fmaf(bfhi(kr[i + 3][p]), bflo(wr[p][2]), a1);
        unsigned ks = pack2(a0, a1);
        o[p] = pack2(bflo(ks) * bflo(vr[i][p]), bfhi(ks) * bfhi(vr[i][p]));
      }
      *(u32x4*)(smem + (63 - (2 * r2 + i)) * TRS + c8 * 16) = o;
    }
    {
      char* qo = ws + WS_QG + (size_t)(ch >> 2) * (S_ * 8) + sA * 8;
      st16(qo, (u32x4){qr[0][0], qr[0][1], qr[1][0], qr[1][1]});
      st16(qo + S_ * 8, (u32x4){qr[0][2], qr[0][3], qr[1][2], qr[1][3]});
    }
    __syncthreads();
    STAMPV(1);
    {
      const int g = lane >> 4, q = (lane >> 2) & 3, p = lane & 3;
      const unsigned base = (unsigned)(size_t)(lds_cp)smem + (8 * g + q) * TRS + wid * 32 + p * 8;
      const int d = dt * 64 + wid * 16 + (lane & 15);
      u16* kvo = (u16*)(ws + WS_KVR) + (size_t)d * S_ + (1984 - 64 * st) + 8 * g;
#pragma unroll
      for (int hf = 0; hf < 2; ++hf) {
        u32x2 a = trread(base + hf * 32 * TRS), b = trread(base + (hf * 32 + 4) * TRS);
        st16(kvo + hf * 32, (u32x4){a[0], a[1], b[0], b[1]});
      }
    }
    STAMPV(2); STAMPDRAIN; STAMPV(3); STAMPOUT(3, 4, tb);
  } else if (bid < NTILE + NMOD) {
    STAMPDECL STAMPV(0);
    const int nb = (bid - NTILE) % 54, kp = (bid - NTILE) / 54;
    const int cg = tid & 7, rg = kp * 192 + (tid >> 3);
    u32x4 wv[6];
    u16 cr[6];
#pragma unroll
    for (int i = 0; i < 6; ++i) wv[i] = *(const u32x4*)(wmod + (size_t)(rg + 32 * i) * 3456 + nb * 64 + cg * 8);
#pragma unroll
    for (int i = 0; i < 6; ++i) cr[i] = cvec[rg + 32 * i];
    SCHED0;
    float a[8];
#pragma unroll
    for (int j = 0; j < 8; ++j) a[j] = 0.f;
#pragma unroll
    for (int i = 0; i < 6; ++i) {
      float cv = bf2f(cr[i]);
      float sv = rbf(cv / (1.f + __expf(-cv)));
#pragma unroll
      for (int p = 0; p < 4; ++p) { a[2 * p] += sv * bflo(wv[i][p]); a[2 * p + 1] += sv * bfhi(wv[i][p]); }
    }
    float* red = (float*)smem;
    *(f32x4*)(red + (tid >> 3) * 68 + cg * 8) = (f32x4){a[0], a[1], a[2], a[3]};
    *(f32x4*)(red + (tid >> 3) * 68 + cg * 8 + 4) = (f32x4){a[4], a[5], a[6], a[7]};
    __syncthreads();
    {
      const int col = tid >> 2, part = tid & 3;
      float r = 0.f;
#pragma unroll
      for (int i = 0; i < 8; ++i) r += red[(part * 8 + i) * 68 + col];
      r += __shfl_xor(r, 1); r += __shfl_xor(r, 2);
      if (part == 0) st4((float*)(ws + WS_MODP) + kp * 3456 + nb * 64 + col, __float_as_uint(r));
    }
    STAMPV(1); STAMPOUT(5, 2, bid - NTILE);
  } else {
    const int sb = bid - NMOD - NTILE;
    if (wid < 2) {
      const int row = sb * 16 + wid * 8 + (lane >> 3), c = lane & 7;
      u32x4 v[9];
#pragma unroll
      for (int i = 0; i < 9; ++i) v[i] = *(const u32x4*)(Aq + (size_t)row * D_ + (c + 8 * i) * 8);
      float s1 = 0.f;
#pragma unroll
      for (int i = 0; i < 9; ++i)
#pragma unroll
        for (int p = 0; p < 4; ++p) s1 = dot2(v[i][p], 0x3f803f80u, s1);
      s1 += __shfl_xor(s1, 1); s1 += __shfl_xor(s1, 2); s1 += __shfl_xor(s1, 4);
      const float mean = s1 * (1.f / D_);
      float s2 = 0.f;
#pragma unroll
      for (int i = 0; i < 9; ++i)
#pragma unroll
        for (int p = 0; p < 4; ++p) {
          float a = bflo(v[i][p]) - mean, b = bfhi(v[i][p]) - mean;
          s2 = fmaf(a, a, s2); s2 = fmaf(b, b, s2);
        }
      s2 += __shfl_xor(s2, 1); s2 += __shfl_xor(s2, 2); s2 += __shfl_xor(s2, 4);
      if (c == 0) {
        float sd = rbf(sqrtf(rbf(rbf(s2 * (1.f / D_)) + 1e-5f)));
        const float rs = 1.f / sd;
        st8(ws + WS_ST1 + row * 8, (u32x2){__float_as_uint(rs), __float_as_uint(-rbf(mean) * rs)});
      }
    } else {
      if (wid == 2 && lane < 32) st16(ws + WS_FLAG + sb * 512 + lane * 16, (u32x4){0u, 0u, 0u, 0u});
      if (wid == 3 && lane < 8) st16(ws + WS_ST2 + sb * 128 + lane * 16, (u32x4){0u, 0u, 0u, 0u});
      wait_vm<0>();
    }
  }
  keep(pf);
}

constexpr int NCONV = 144, NUV = 108;
#define EPL 1
#define CVO 4
constexpr int KCS = 4160;
constexpr int KER_BYTES = 4 * KCS;
constexpr int RS = 4608;
constexpr int YS = 4096 + 64;
constexpr int CB_R = 4 * KER_BYTES;
constexpr int CB_Y = CB_R + 4 * RS;
constexpr int CB_X = CB_Y + 4 * YS;
constexpr int CONV_LDS = CB_X + 32768;
DI float modv(const char* ws, int n) {
  const float* m = (const float*)(ws + WS_MODP);
  return rbf((m[n] + m[3456 + n]) + m[2 * 3456 + n]);
}
constexpr int tri_start(int u) { return 8 * u - u * (u - 1) / 2; }
constexpr int tri_row(int n) { int u = 0; while (tri_start(u + 1) <= n) ++u; return u; }

extern "C" __global__ __launch_bounds__(512, 1)
void k_conv(const u16* __restrict__ ssmk, const u16* __restrict__ ssmD, const u16* __restrict__ Wg,
            const u16* __restrict__ Wu, const u16* __restrict__ Wd, char* __restrict__ ws) {
  extern __shared__ __attribute__((aligned(16))) char smem[];
  const int tid = threadIdx.x, lane = tid & 63;
  const int wid = __builtin_amdgcn_readfirstlane(tid >> 6);
  const int bid = blockIdx.x;
  unsigned pf;
  CODEPF(pf, "k_conv", "k_gemm2", wid, lane);
  ssmk = gpin(ssmk); ssmD = gpin(ssmD); Wg = gpin(Wg); Wu = gpin(Wu); ws = gpin(ws);
  if (bid < NCONV) {
    STAMPDECL STAMPV(0);
    const unsigned lds0 = (unsigned)(size_t)(lds_cp)smem;
    const int grp = (bid & 7) * 18 + (bid >> 3);
    const int c0 = grp * 4;
    const int cw = wid & 3, hw = wid >> 2;
    f32x2 st1[4];
    u32x2 q4[4];
    float mp[3];
    float Dv;
    auto epload = [&]() {
      const int mj = lane >> 2;
      const float* m = (const float*)(ws + WS_MODP) + (mj < 3 ? mj : mj == 3 ? 2 : 4) * D_ + c0 + (lane & 3);
#pragma unroll
      for (int k = 0; k < 3; ++k) mp[k] = m[k * 3456];
#pragma unroll
      for (int i = 0; i < 4; ++i) {
        st1[i] = *(const f32x2*)(ws + WS_ST1 + (size_t)(512 * i + tid) * 8);
        q4[i] = *(const u32x2*)(ws + WS_QG + (size_t)grp * (S_ * 8) + (512 * i + tid) * 8);
      }
      SCHED0;
    };
    {
      const u16* kerg = ssmk + (size_t)(c0 + cw) * S_;
      char* kb = smem + cw * KER_BYTES;
      u32x4 a[2]; u32x2 b[2];
#pragma unroll
      for (int i = 0; i < 2; ++i) {
        int ch = (hw * 2 + i) * 64 + lane;
        a[i] = *(const u32x4*)(kerg + ch * 8);
        b[i] = *(const u32x2*)(kerg + (ch < 255 ? ch * 8 + 8 : 0));
      }
      SCHED0;
      {
        const char* kvsrc = ws + WS_KVR + (size_t)(c0 + cw) * 4096 + hw * 2048;
        const unsigned vo = lane * 16;
        dma1k<0>(kvsrc, vo, lds0 + CB_R + cw * RS + hw * 2048);
        dma1k<1024>(kvsrc, vo + 1024, lds0 + CB_R + cw * RS + hw * 2048);
      }
      Dv = bf2f(ssmD[c0 + cw]);
      SCHED0;
      if (hw == 1 && lane == 63) b[1] = (u32x2){0u, 0u};
#pragma unroll
      for (int i = 0; i < 2; ++i) {
        int ch = (hw * 2 + i) * 64 + lane;
        u32x4 c1, c2, c3;
        c1[0] = __builtin_amdgcn_alignbit(a[i][1], a[i][0], 16); c1[1] = __builtin_amdgcn_alignbit(a[i][2], a[i][1], 16);
        c1[2] = __builtin_amdgcn_alignbit(a[i][3], a[i][2], 16); c1[3] = __builtin_amdgcn_alignbit(b[i][0], a[i][3], 16);
        c2[0] = a[i][1]; c2[1] = a[i][2]; c2[2] = a[i][3]; c2[3] = b[i][0];
        c3[0] = c1[1]; c3[1] = c1[2]; c3[2] = c1[3]; c3[3] = __builtin_amdgcn_alignbit(b[i][1], b[i][0], 16);
        int off = (ch * 8 + 16) * 2;
        *(u32x4*)(kb + off) = a[i];
        *(u32x4*)(kb + KCS + off) = c1;
        *(u32x4*)(kb + 2 * KCS + off) = c2;
        *(u32x4*)(kb + 3 * KCS + off) = c3;
        if (ch == 0) {
          u32x4 z = {0u, 0u, 0u, 0u};
          *(u32x4*)(kb) = z; *(u32x4*)(kb + KCS) = z; *(u32x4*)(kb + 2 * KCS) = z; *(u32x4*)(kb + 3 * KCS) = z;
          u32x4 e1 = z, e2 = z, e3 = z;
          e1[3] = a[i][0] << 16;
          e2[3] = a[i][0];
          e3[2] = a[i][0] << 16; e3[3] = c1[0];
          *(u32x4*)(kb + 16) = z;
          *(u32x4*)(kb + KCS + 16) = e1;
          *(u32x4*)(kb + 2 * KCS + 16) = e2;
          *(u32x4*)(kb + 3 * KCS + 16) = e3;
        }
      }
      if (hw == 1 && lane < 32) *(u32x4*)(smem + CB_R + cw * RS + 4096 + lane * 16) = (u32x4){0u, 0u, 0u, 0u};
    }
    wait_vm<(EPL == 0) ? 11 : 0>();
    __builtin_amdgcn_s_waitcnt(0xC07F);
    __builtin_amdgcn_s_barrier();
    SCHED0;
    STAMPV(1);
    epload();
    float xf[4][4], hh[4][4], cA[4], cB[4], cb1[4], mv, mt;
    u32x2 kv[4];
    unsigned wm = 0;
    const unsigned wo = (unsigned)(bid * 288 + (tid < 287 ? tid : 287)) * 128u;
    f32x4 acc[8];
#pragma unroll
    for (int t = 0; t < 8; ++t) acc[t] = (f32x4){0.f, 0.f, 0.f, 0.f};
    const int b = lane & 15, g = lane >> 4;
    {
      typedef __attribute__((address_space(3))) const u32x2* lp2;
      typedef __attribute__((address_space(3))) const bf16x8* lp4;
      const int x0 = 1 + b + 8 * g;
      unsigned a_lo = lds0 + cw * KER_BYTES + (x0 & 3) * KCS + (x0 & ~3) * 2 + hw * 256, a_hi = a_lo + 8;
      unsigned gb = lds0 + CB_R + cw * RS + (2032 - 16 * b + 8 * g) * 2 - 7 * 512 + hw * 256;
      asm volatile("" : "+v"(a_lo), "+v"(a_hi), "+v"(gb));
      bf16x8 G[2][8];
      u32x2 Al[2][8], Ah[2][8];
      const unsigned kvo = lds0 + CB_R + cw * RS + (2044 - 1024 * hw - 16 * b - 4 * g) * 2;
      const float one = ((lane >> 2) == 1 || lane >= 16) ? 1.f : 0.f;
      auto ld = [&](auto uc, auto jc) {
        constexpr int u0 = decltype(uc)::value, j = decltype(jc)::value, bf = u0 & 1, i = j < 2 ? 0 : (j - 8) >> 1;
        if constexpr (j >= 2 && j < 10) G[bf][j - 2] = *(lp4)(size_t)(gb + u0 * 64 + (9 - j) * 512);
        else if constexpr ((j & 1) == 0) Al[bf][i] = *(lp2)(size_t)(a_lo + u0 * 64 + i * 512);
        else Ah[bf][i] = *(lp2)(size_t)(a_hi + u0 * 64 + i * 512);
        SCHED0;
      };
      static_for<0, 24>([&](auto jc) { ld(std::integral_constant<int, 0>{}, jc); });
      static_for<0, 4>([&](auto uc) {
        constexpr int u0 = decltype(uc)::value, cur = u0 & 1;
        static_for<0, 36>([&](auto nc) {
          constexpr int n = decltype(nc)::value;
          constexpr int u1 = tri_row(n), t = u1 + (n - tri_start(u1));
          bf16x8 A = __builtin_bit_cast(bf16x8, (u32x4){Al[cur][u1][0], Al[cur][u1][1], Ah[cur][u1][0], Ah[cur][u1][1]});
          acc[t] = __builtin_amdgcn_mfma_f32_16x16x32_bf16(A, G[cur][t - u1], acc[t], 0, 0, 0);
          SCHED0;
          if constexpr (u0 < 3 && n >= CVO && n < CVO + 24) ld(std::integral_constant<int, u0 + 1>{}, std::integral_constant<int, n - CVO>{});
          if constexpr (u0 == 3) {
            if constexpr (n < 4) kv[n] = *(lp2)(size_t)(kvo - n * 512);
            else if constexpr (n == 4) { mv = rbf((mp[0] + mp[1]) + mp[2]); asm volatile("" : "+v"(mv)); }
            else if constexpr (n == 5) { mv = rbf(mv + one); asm volatile("" : "+v"(mv)); }
            else if constexpr (n == 6) {
              mt = mv * __int_as_float(__builtin_amdgcn_update_dpp(0, __float_as_int(mv), 0x128, 0xf, 0xf, false));
              asm volatile("" : "+v"(mt));
            } else if constexpr (n >= 8 && n < 12) {
              constexpr int c = n - 8;
              cB[c] = __int_as_float(__builtin_amdgcn_readlane(__float_as_int(mt), c));
              cA[c] = __int_as_float(__builtin_amdgcn_readlane(__float_as_int(mt), 4 + c));
              cb1[c] = __int_as_float(__builtin_amdgcn_readlane(__float_as_int(mv), 16 + c));
              asm volatile("" : "+v"(cB[c]), "+s"(cA[c]), "+s"(cb1[c]));
            } else if constexpr (n >= 14 && n < 30) {
              constexpr int i = (n - 14) >> 2, c = (n - 14) & 3;
              xf[i][c] = (c & 1) ? bfhi(q4[i][c >> 1]) : bflo(q4[i][c >> 1]);
              hh[i][c] = fmaf(fmaf(xf[i][c], st1[i][0], st1[i][1]), cA[c], cB[c]);
              asm volatile("" : "+v"(xf[i][c]), "+v"(hh[i][c]));
            } else if constexpr (n == 31) {
              asm volatile("global_load_dword %0, %1, %2" : "=v"(wm) : "v"(wo), "s"(Wd) : "memory");
            }
            SCHED0;
          }
        });
      });
    }
    STAMPV(2);
    auto fin = [&](auto hc) {
      constexpr int H = decltype(hc)::value;
      char* xb = smem + CB_X + wid * 4096 + lane * 16;
#pragma unroll
      for (int t = 0; t < 4; ++t) *(f32x4*)(xb + t * 1024) = acc[4 * (H ^ 1) + t];
      __syncthreads();
      const char* xr = smem + CB_X + (wid ^ 4) * 4096 + lane * 16;
      f32x4 o[4];
#pragma unroll
      for (int t = 0; t < 4; ++t) o[t] = *(const f32x4*)(xr + t * 1024);
      SCHED0;
#pragma unroll
      for (int t = 0; t < 4; ++t) {
        const f32x4 y = acc[4 * H + t];
        const float y0 = fmaf(bfhi(kv[t][1]), Dv, y[0]) + o[t][0], y1 = fmaf(bflo(kv[t][1]), Dv, y[1]) + o[t][1];
        const float y2 = fmaf(bfhi(kv[t][0]), Dv, y[2]) + o[t][2], y3 = fmaf(bflo(kv[t][0]), Dv, y[3]) + o[t][3];
        *(u32x2*)(smem + CB_Y + cw * YS + (256 * (H * 4 + t) + 16 * b + 4 * g) * 2) = (u32x2){pack2(y0, y1), pack2(y2, y3)};
      }
    };
    if (hw == 0) fin(std::integral_constant<int, 0>{});
    else fin(std::integral_constant<int, 1>{});
    __syncthreads();
    STAMPV(3);
    {
      const int q = (lane >> 2) & 3, p = lane & 3;
      const unsigned toff = q * YS + (wid * 64 + 16 * g + 4 * p) * 2;
      char* zo = ws + WS_XF + (size_t)(c0 >> 4) * 1024 + ((c0 >> 3) & 1) * 512 + ((c0 >> 2) & 1) * 8;
      const unsigned vo = (tid >> 5) * 36 * 1024 + (tid & 31) * 16;
      u32x2 y4[4];
#pragma unroll
      for (int i = 0; i < 4; ++i) y4[i] = trread(lds0 + CB_Y + toff + i * 1024);
      SCHED0;
#pragma unroll
      for (int i = 0; i < 4; ++i) {
        float x1[4];
#pragma unroll
        for (int c = 0; c < 4; ++c) x1[c] = fmaf(hh[i][c], (c & 1) ? bfhi(y4[i][c >> 1]) : bflo(y4[i][c >> 1]), xf[i][c]);
        gstore8c(zo + i * (16 * 36 * 1024), vo, (u32x2){pack2(x1[0] * cb1[0], x1[1] * cb1[1]), pack2(x1[2] * cb1[2], x1[3] * cb1[3])});
        gstore8c(zo + SD2 + i * (16 * 36 * 1024), vo, (u32x2){pack2(x1[0], x1[1]), pack2(x1[2], x1[3])});
      }
    }
    STAMPV(4);
    wait_vm<0>();
    keep(wm);
    STAMPV(5); STAMPOUT(2, 6, bid);
  } else if (bid < NCONV + NUV) {
    STAMPDECL STAMPV(0);
#define UVSLEEP 16
    __builtin_amdgcn_s_sleep(UVSLEEP);
    const int ub = bid - NCONV;
    const int kp = ub / 36, cbk = ub % 36;
    const int mat = cbk / 18, col0 = (cbk % 18) * 256;
    const u16* W = mat ? Wu : Wg;
    const int cg = tid & 31, rg = tid >> 5;
    u32x4 wv[12];
    const float* mp = (const float*)(ws + WS_MODP) + 3 * D_ + (tid >= 192 ? D_ - 192 : 0) + kp * 192 + (tid < 384 ? tid : 383);
    const float m0 = mp[0], m1 = mp[3456], m2 = mp[2 * 3456];
    SCHED0;
#pragma unroll
    for (int i = 0; i < 12; ++i)
      wv[i] = *(const u32x4*)(W + (size_t)(kp * 192 + 2 * rg + (i & 1) + 32 * (i >> 1)) * F_ + col0 + cg * 8);
    SCHED0;
    const float mv = rbf((m0 + m1) + m2);
    ((u16*)smem)[tid] = (u16)pack2(tid >= 192 ? 1.f + mv : mv, 0.f);
    __syncthreads();
    float a1[8], a2[8];
#pragma unroll
    for (int j = 0; j < 8; ++j) { a1[j] = 0.f; a2[j] = 0.f; }
#pragma unroll
    for (int i = 0; i < 6; ++i) {
      const unsigned sbv = *(const unsigned*)(smem + (2 * rg + 32 * i) * 2);
      const unsigned c1 = *(const unsigned*)(smem + (192 + 2 * rg + 32 * i) * 2);
#pragma unroll
      for (int p = 0; p < 4; ++p) {
        unsigned lo = __builtin_amdgcn_perm(wv[2 * i + 1][p], wv[2 * i][p], 0x05040100u);
        unsigned hi = __builtin_amdgcn_perm(wv[2 * i + 1][p], wv[2 * i][p], 0x07060302u);
        a1[2 * p] = dot2(lo, c1, a1[2 * p]); a1[2 * p + 1] = dot2(hi, c1, a1[2 * p + 1]);
        a2[2 * p] = dot2(lo, sbv, a2[2 * p]); a2[2 * p + 1] = dot2(hi, sbv, a2[2 * p + 1]);
      }
    }
    float* red = (float*)(smem + 1024);
#pragma unroll
    for (int j = 0; j < 4; ++j)
      *(f32x4*)(red + rg * 512 + (j * 32 + cg) * 4) = (f32x4){a1[2 * j], a2[2 * j], a1[2 * j + 1], a2[2 * j + 1]};
    __syncthreads();
    {
      float r = 0.f;
#pragma unroll
      for (int w = 0; w < 16; ++w) r += red[w * 512 + tid];
      const int j = tid >> 7, cgo = (tid >> 2) & 31, e = tid & 3;
      const int f = col0 + cgo * 8 + 2 * j + (e >> 1);
      char* uo = ws + WS_UF + (f >> 4) * 1024 + ((kp >> 1) * 32 + mat * 16 + (f & 15)) * 16 + (e & 1) * 4;
      st4(uo + (kp & 1) * 8, pack2(r, r - rbf(r)));
      if (kp == 2) st4(uo + 8, 0u);
    }
    STAMPV(1); STAMPOUT(4, 2, ub);
  } else {
    float g0 = modv(ws, 5 * D_ + tid), g1 = modv(ws, 5 * D_ + 512 + lane);
    st4((float*)(ws + WS_GB) + tid, __float_as_uint(g0));
    if (tid < 64) st4((float*)(ws + WS_GB) + 512 + tid, __float_as_uint(g1));
  }
  keep(pf);
}

template <int OFF> DI void gload16(bf16x8& dst, const char* sbase, unsigned voff) {
  asm volatile("global_load_dwordx4 %0, %1, %2 offset:%3" : "=v"(dst) : "v"(voff), "s"(sbase), "i"(OFF) : "memory");
}
template <int OFF> DI void gload16a(bf16x8& dst, const char* sbase, unsigned voff) {
  asm volatile("global_load_dwordx4 %0, %1, %2 offset:%3" : "=a"(dst) : "v"(voff), "s"(sbase), "i"(OFF) : "memory");
}
template <int OFF> DI void gload8_sc1(u32x2& dst, const char* sbase, unsigned voff) {
  asm volatile("global_load_dwordx2 %0, %1, %2 offset:%3 sc1" : "=v"(dst) : "v"(voff), "s"(sbase), "i"(OFF) : "memory");
}
template <int OFF> DI void gload16_sc1(u32x4& dst, const char* sbase, unsigned voff) {
  asm volatile("global_load_dwordx4 %0, %1, %2 offset:%3 sc1" : "=v"(dst) : "v"(voff), "s"(sbase), "i"(OFF) : "memory");
}
template <int OFF> DI void gstore16_sc1(char* sbase, unsigned voff, const u32x4& v) {
  asm volatile("global_store_dwordx4 %0, %1, %2 offset:%3 sc1\n\ts_nop 1" ::"v"(voff), "v"(v), "s"(sbase), "i"(OFF) : "memory");
}
DI void gstore8_sc1(char* sbase, unsigned voff, const u32x2& v) {
  asm volatile("global_store_dwordx2 %0, %1, %2 sc1" ::"v"(voff), "v"(v), "s"(sbase) : "memory");
}
DI void pin4(f32x4& v) { asm volatile("" : "+v"(v)); }
DI void pin4u(u32x4& v) { asm volatile("" : "+v"(v)); }
DI void pin2u(u32x2& v) { asm volatile("" : "+v"(v)); }
DI void pinb(bf16x8& v) { asm volatile("" : "+v"(v)); }
DI void pinba(bf16x8& v) { asm volatile("" : "+a"(v)); }
DI bf16x8 trfrag(unsigned addr_lo, unsigned addr_hi) {
  s16x4 lo = __builtin_amdgcn_ds_read_tr16_b64_v4i16((lds_s16x4p)(size_t)addr_lo);
  s16x4 hi = __builtin_amdgcn_ds_read_tr16_b64_v4i16((lds_s16x4p)(size_t)addr_hi);
  return __builtin_shufflevector(lo, hi, 0, 1, 2, 3, 4, 5, 6, 7);
}
template <int N> DI void wait_lgkm() { __builtin_amdgcn_s_waitcnt(0xC07F | (N << 8)); }
DI s16x4 trhalf(unsigned addr) { return __builtin_amdgcn_ds_read_tr16_b64_v4i16((lds_s16x4p)(size_t)addr); }
DI bf16x8 join(s16x4 lo, s16x4 hi) { return __builtin_shufflevector(lo, hi, 0, 1, 2, 3, 4, 5, 6, 7); }
template <int P> DI void dma_piece(const char* wsrc, const unsigned (&voff)[9], unsigned wdst) {
  dma1k<P * 1024>(wsrc, voff[P], wdst);
}
#define DMA9(SRC, DST) do { dma_piece<0>(SRC, voff, DST); dma_piece<1>(SRC, voff, DST); dma_piece<2>(SRC, voff, DST); \
    dma_piece<3>(SRC, voff, DST); dma_piece<4>(SRC, voff, DST); dma_piece<5>(SRC, voff, DST); \
    dma_piece<6>(SRC, voff, DST); dma_piece<7>(SRC, voff, DST); dma_piece<8>(SRC, voff, DST); } while (0)

constexpr int opc(int k, int t, int i) { return k * 10000 + t * 100 + i; }
constexpr int op_n(int op) { int k = op / 10000; return k == 0 ? 0 : (k == 2 || k == 6 || k == 8) ? 2 : k == 3 ? 3 : 1; }
constexpr int w3222(int t, int i, int f) {
  int first = (i == 0) ? 0 : 2 * i + 1, cnt = (i == 0) ? 3 : 2;
  return (f % 3 == 1 && f / 3 < cnt) ? opc(1, t, first + f / 3) : 0;
}
template <typename P> constexpr int since(int op, int s, int i) {
  int n = -1;
  for (int a = -1; a <= s; ++a)
    for (int b = 0; b < 4; ++b) {
      if (a == s && b == i) return n;
      for (int c = 0; c < 12; ++c) {
        int o = P::plan(a, b, c);
        if (n >= 0) n += op_n(o);
        if (o == op) n = 0;
      }
    }
  return n;
}
constexpr int cmin(int a, int b) { return a < b ? a : b; }

constexpr int G1_UP = 18432 + 128;
constexpr int SLOT1 = 37120;
constexpr int G1_UF = 4 * SLOT1;
constexpr int GEMM1_LDS = G1_UF + 9216;
#define NA 5
struct P1 {
  static constexpr int plan(int s, int i, int c) {
    if (s == -1) return i == 0 ? (c < 9 ? opc(1, 0, c) : 0) : i == 1 ? (c < 4 ? opc(2, 0, c) : c == 4 ? opc(7, 0, 0) : 0) : 0;
    if (s == 0) {
      if (c == 9) return opc(2, 1, i);
      if (c == 10) return i == 0 ? opc(3, 0, 0) : 0;
      if (c == 11) return 0;
      if (i == 0) return opc(1, 1, c);
      if (i == 1) return (c & 1) ? 0 : opc(1, 2, c >> 1);
      if (i == 2) return (c & 1) ? opc(1, 2, 5 + (c >> 1)) : 0;
      return c < 3 ? opc(4, 0, c) : c == 8 ? opc(5, 0, 0) : 0;
    }
    if (s == 1) {
      if (c == 9) return opc(2, 2, i);
      if (c > 9) return 0;
      if (i & 1) return (c & 1) ? opc(1, 3 + (i >> 1), 5 + (c >> 1)) : 0;
      return (c & 1) ? 0 : opc(1, 3 + (i >> 1), c >> 1);
    }
    if (s <= 5) return c == 9 ? opc(2, s + 1, i) : c < 9 ? w3222(s + 3, i, c) : 0;
    if (s == 6) return i == 0 ? (c < 8 ? opc(2, 7 + (c >> 2), c & 3) : c == 8 ? opc(6, 0, 0) : c == 9 ? opc(7, 0, 1) : c == 10 ? opc(8, 0, 0) : 0) : 0;
    return 0;
  }
  static constexpr int wait1(int s, int i) {
    int w = since<P1>(opc(2, s, i), s, i);
    if (i == 3) w = cmin(w, since<P1>(opc(1, s + 1, 8), s, i));
    if (i == 3 && s == 0) w = cmin(w, since<P1>(opc(3, 0, 0), s, i));
    if (i == 3 && s == 6) w = cmin(w, since<P1>(opc(1, 8, 8), s, i));
    return w;
  }
  static constexpr int wait(int s, int i) {
    if (s != 2) return wait1(s, i);
    int w = 63;
    for (int a = 2; a <= 5; ++a) w = cmin(w, wait1(a, i));
    return w;
  }
};
struct G1State {
  f32x16 acc[2][9];
  bf16x8 xr[4][2];
  bf16x8 wf[2][9];
  bf16x8 xt[8][2];
  bf16x8 sv[3];
  u32x2 sg[2];
  float s1, s2;
  unsigned pf[4];
};
struct G1Ptr {
  const char *wbase, *xbase, *sx, *ufsrc, *sgsrc, *wdsrc;
  char* stdst;
  unsigned voff[9];
  unsigned xoff0, xoff1, so, wdst0, lds0, sgoff;
  int wid, lane;
};
template <int F, int Z> DI void g1_mfma(f32x16& c, const bf16x8& a, const bf16x8& b) {
  if constexpr (Z) {
    if constexpr (F < NA) asm volatile("v_mfma_f32_32x32x16_bf16 %0, %1, %2, 0" : "=a"(c) : "v"(a), "v"(b));
    else asm volatile("v_mfma_f32_32x32x16_bf16 %0, %1, %2, 0" : "=&v"(c) : "v"(a), "v"(b));
  } else {
    if constexpr (F < NA) asm volatile("v_mfma_f32_32x32x16_bf16 %0, %1, %2, %0" : "+a"(c) : "v"(a), "v"(b));
    else asm volatile("v_mfma_f32_32x32x16_bf16 %0, %1, %2, %0" : "+v"(c) : "v"(a), "v"(b));
  }
}
template <int F> DI void g1_mfma_ba(f32x16& c, const bf16x8& a, const bf16x8& b) {
  if constexpr (F < NA) asm volatile("v_mfma_f32_32x32x16_bf16 %0, %1, %2, %0" : "+a"(c) : "v"(a), "a"(b));
  else asm volatile("v_mfma_f32_32x32x16_bf16 %0, %1, %2, %0" : "+v"(c) : "v"(a), "a"(b));
}
template <int F> DI void g1_pin(f32x16& c0, f32x16& c1) {
  if constexpr (F < NA) asm volatile("" : "+a"(c0), "+a"(c1));
  else asm volatile("" : "+v"(c0), "+v"(c1));
}
template <int F> DI float g1_get(const f32x16& c, int r) {
  if constexpr (F < NA) {
    float x;
    asm volatile("v_accvgpr_read_b32 %0, %1" : "=v"(x) : "a"(c[r]));
    return x;
  } else return c[r];
}
template <int I, int F> DI bf16x8 g1_frag(unsigned rd) {
  return trfrag(rd + (16 * I) * 288 + 32 * F, rd + (16 * I + 4) * 288 + 32 * F);
}
template <int I, int F> DI bf16x8 g1_lastfrag(unsigned rd7, unsigned rd8, unsigned ufrd) {
  if constexpr (I < 4) return g1_frag<I, F>(rd7);
  else if constexpr (I < 8) return g1_frag<I - 4, F>(rd8);
  else return *(const __attribute__((address_space(3))) bf16x8*)(size_t)(ufrd + F * 1024);
}
DI float dpp_ror_add(float v, int) { return v; }
template <int CTRL> DI float dpp_add(float v) {
  return v + __int_as_float(__builtin_amdgcn_update_dpp(0, __float_as_int(v), CTRL, 0xf, 0xf, false));
}
DI float swap16_add(float v) {
  auto r = __builtin_amdgcn_permlane16_swap(__float_as_uint(v), __float_as_uint(v), false, false);
  return __uint_as_float(r[0]) + __uint_as_float(r[1]);
}
DI float swap32_add(float v) {
  auto r = __builtin_amdgcn_permlane32_swap(__float_as_uint(v), __float_as_uint(v), false, false);
  return __uint_as_float(r[0]) + __uint_as_float(r[1]);
}
template <int C> DI void g1_stats(G1State& st, const G1Ptr& p) {
  if constexpr (C < 3) {
    constexpr int j = (C + 2) % 3;
    if constexpr (C == 0) { st.s1 = 0.f; st.s2 = 0.f; }
    u32x4 t = __builtin_bit_cast(u32x4, st.sv[j]);
#pragma unroll
    for (int q = 0; q < 4; ++q) { st.s1 = dot2(t[q], 0x3f803f80u, st.s1); st.s2 = dot2(t[q], t[q], st.s2); }
    if constexpr (C == 0) { if (p.lane >= 16) { st.s1 = 0.f; st.s2 = 0.f; } }
  } else if constexpr (C == 3) {
    st.s1 = dpp_add<0x122>(st.s1); st.s2 = dpp_add<0x122>(st.s2);
    st.s1 = dpp_add<0x124>(st.s1); st.s2 = dpp_add<0x124>(st.s2);
  } else if constexpr (C == 4) {
    st.s1 = dpp_add<0x128>(st.s1); st.s2 = dpp_add<0x128>(st.s2);
  } else if constexpr (C == 5) {
    st.s1 = swap16_add(st.s1); st.s2 = swap16_add(st.s2);
  } else if constexpr (C == 6) {
    st.s1 = swap32_add(st.s1); st.s2 = swap32_add(st.s2);
  } else if constexpr (C == 7) {
    st.s1 = st.s1 * (1.f / D_);
    st.s2 = rbf(rbf(fmaxf(st.s2 * (1.f / D_) - st.s1 * st.s1, 0.f)) + 1e-5f);
  } else {
    const float sd = __builtin_amdgcn_sqrtf(st.s2);
    u32x2 gr = {pack2(-st.s1, -st.s1), pack2(sd, sd)};
    gstore8_sc1(p.stdst, (p.lane & 1) * 8, gr);
  }
}
constexpr size_t WSTEP1 = (size_t)64 * F_ * 2;
template <int S, int OP>
DI void g1_op(G1State& st, const G1Ptr& p, const char* wsrc, unsigned wdst, const char* xsrc) {
  constexpr int k = OP / 10000, t = (OP / 100) % 100, i = OP % 100;
  if constexpr (k == 1) {
    if constexpr (S == 2) dma_piece<i>(wsrc, p.voff, wdst);
    else dma_piece<i>(p.wbase + t * WSTEP1, p.voff, p.wdst0 + (t & 3) * SLOT1);
  } else if constexpr (k == 2) {
    if constexpr (S == 6) {
      gload16a<i * 1024>(st.xt[(t - 7) * 4 + i][0], p.xbase + t * 4096, p.xoff0);
      gload16a<i * 1024>(st.xt[(t - 7) * 4 + i][1], p.xbase + t * 4096, p.xoff1);
    } else {
      gload16<i * 1024>(st.xr[i][0], xsrc, p.xoff0); gload16<i * 1024>(st.xr[i][1], xsrc, p.xoff1);
    }
  } else if constexpr (k == 3) {
    gload16<0>(st.sv[0], p.sx, p.so);
    gload16<0>(st.sv[1], p.sx, p.so + 16384);
    gload16<0>(st.sv[2], p.sx, (p.so & 4095) + 32768);
  } else if constexpr (k == 4) {
    int q = p.wid + 4 * i; q = q > 8 ? 8 : q;
    dma1k<G1_UF>(p.ufsrc + q * 1024, p.xoff0, p.lds0 + q * 1024);
  } else if constexpr (k == 6) {
    gload8_sc1<0>(st.sg[0], p.sgsrc, p.sgoff); gload8_sc1<256>(st.sg[1], p.sgsrc, p.sgoff);
  } else if constexpr (k == 8) {
    asm volatile("global_load_dword %0, %2, %4\n\tglobal_load_dword %1, %3, %4" : "=&v"(st.pf[2]), "=&v"(st.pf[3])
                 : "v"(p.xoff0 * 8), "v"((p.lane < 16 ? p.lane : 16) * 128 + 8192), "s"(p.wdsrc) : "memory");
  } else if constexpr (k == 7) {
    if constexpr (i == 0) CODEPF(st.pf[0], "k_gemm1", "k_gemm2", p.wid, p.lane);
    else CODEPF(st.pf[1], "k_gemm2", "k_zend", p.wid, p.lane);
  }
}
template <int S>
DI void g1_super(G1State& st, const G1Ptr& p, unsigned rd, unsigned rd_next, unsigned rd_next2, const char* wsrc,
                 unsigned wdst, const char* xsrc) {
  static_for<0, 4>([&](auto ic) {
    constexpr int i = decltype(ic)::value;
    constexpr int cur = i & 1, nxt = cur ^ 1;
    wait_vm<P1::wait(S, i)>();
    pinb(st.xr[i][0]); pinb(st.xr[i][1]);
    if constexpr (S == 0 && i == 3) { pinb(st.sv[0]); pinb(st.sv[1]); pinb(st.sv[2]); }
    if constexpr (i == 3) {
      wait_lgkm<0>();
      asm volatile("" ::: "memory");
      __builtin_amdgcn_s_barrier();
    }
    SCHED0;
    static_for<0, 9>([&](auto fc) {
      constexpr int f = decltype(fc)::value;
      if constexpr (i != 3 && f == 0) { wait_lgkm<8>(); SCHED0; }
      if constexpr (i != 3 && f == 5) { wait_lgkm<10>(); SCHED0; }
      g1_mfma<f, (S == 0 && i == 0)>(st.acc[0][f], st.wf[cur][f], st.xr[i][0]);
      if constexpr (i < 3) st.wf[nxt][f] = g1_frag<i + 1, f>(rd);
      else if constexpr (S != 6) st.wf[nxt][f] = g1_frag<0, f>(rd_next);
      else st.wf[nxt][f] = g1_lastfrag<f, 0>(rd_next, rd_next2, p.lds0 + G1_UF + p.xoff0);
      SCHED0;
      g1_mfma<f, (S == 0 && i == 0)>(st.acc[1][f], st.wf[cur][f], st.xr[i][1]);
      g1_op<S, P1::plan(S, i, f)>(st, p, wsrc, wdst, xsrc);
      if constexpr (S == 0 && i == 3) g1_stats<f>(st, p);
      SCHED0;
    });
    g1_op<S, P1::plan(S, i, 9)>(st, p, wsrc, wdst, xsrc);
    g1_op<S, P1::plan(S, i, 10)>(st, p, wsrc, wdst, xsrc);
    SCHED0;
  });
}

#define PS1(i) STAMPV(i)
#define WARMF 0
#define HST "sc1"
DI void hstore(char* sbase, unsigned voff, u32x4 v) {
  asm volatile("global_store_dwordx4 %0, %1, %2 " HST "\n\ts_nop 1" ::"v"(voff), "v"(v), "s"(sbase) : "memory");
}
template <int F> DI void g1_epa(const f32x16& c, int r, float& g0, float& g1, float& u0, float& u1) {
  if constexpr (F < NA) {
    asm volatile("v_accvgpr_read_b32 %0, %4\n\tv_accvgpr_read_b32 %1, %5\n\tv_accvgpr_read_b32 %2, %6\n\t"
                 "v_accvgpr_read_b32 %3, %7\n\tv_max_i32 %0, 0, %0"
                 : "=&v"(g0), "=&v"(g1), "=&v"(u0), "=&v"(u1) : "a"(c[r]), "a"(c[r + 1]), "a"(c[r + 8]), "a"(c[r + 9]));
  } else {
    asm volatile("v_max_i32 %0, 0, %2\n\tv_max_i32 %1, 0, %3\n\tv_mul_f32 %0, %0, %4"
                 : "=&v"(g0), "=&v"(g1) : "v"(c[r]), "v"(c[r + 1]), "v"(c[r + 8]));
    u1 = c[r + 9];
  }
}
template <int F> DI unsigned g1_epb(float g0, float g1, float u0, float u1) {
  unsigned o;
  if constexpr (F < NA) {
    asm volatile("v_max_i32 %2, 0, %2\n\tv_mul_f32 %1, %1, %3\n\tv_mul_f32 %2, %2, %4\n\tv_cvt_pk_bf16_f32 %0, %1, %2"
                 : "=v"(o), "+v"(g0), "+v"(g1) : "v"(u0), "v"(u1));
  } else {
    asm volatile("v_mul_f32 %1, %1, %3\n\tv_cvt_pk_bf16_f32 %0, %2, %1" : "=v"(o), "+v"(g1) : "v"(g0), "v"(u1));
  }
  return o;
}

extern "C" __global__ __launch_bounds__(256, 1)
void k_gemm1(const u16* __restrict__ Wg, const u16* __restrict__ Wu, const u16* __restrict__ Wd, char* __restrict__ ws) {
  extern __shared__ __attribute__((aligned(16))) char smem[];
  const int tid = threadIdx.x, lane = tid & 63;
  const int wid = __builtin_amdgcn_readfirstlane(tid >> 6);
  const int bid = blockIdx.x;
  const int xcd = bid & 7, idx = bid >> 3;
  const int cb = (xcd >> 1) * 8 + (idx & 7), rb = (xcd & 1) * 4 + (idx >> 3);
  STAMPDECL STAMPV(0);
  G1Ptr p;
  p.lds0 = (unsigned)(size_t)(lds_cp)smem;
  p.wid = wid; p.lane = lane;
  p.wbase = (const char*)((wid & 2) ? Wu : Wg) + (size_t)(32 * (wid & 1)) * (F_ * 2) + cb * 288;
  p.xbase = ws + WS_XF + (size_t)(rb * 8 + wid * 2) * 36 * 1024;
#pragma unroll
  for (int i = 0; i < 9; ++i) { int c = 64 * i + lane; p.voff[i] = (c / 18) * (F_ * 2) + (c % 18) * 16; }
  p.xoff0 = lane * 16; p.xoff1 = lane * 16 + 36 * 1024;
  p.wdst0 = p.lds0 + (wid >> 1) * G1_UP + (wid & 1) * 9216;
  G1State st;
  static_for<0, 9>([&](auto cc) { g1_op<-1, P1::plan(-1, 0, decltype(cc)::value)>(st, p, nullptr, 0, nullptr); });
  static_for<0, 5>([&](auto cc) { g1_op<-1, P1::plan(-1, 1, decltype(cc)::value)>(st, p, nullptr, 0, p.xbase); });
  SCHED0;
  const int srow = rb * 256 + cb * 8 + wid * 2;
  p.sx = ws + WS_X1F + (size_t)(srow >> 5) * 36 * 1024 + (srow & 31) * 16;
  p.so = (lane >> 1) * 512 + (lane & 1) * 16;
  p.stdst = ws + WS_ST2 + srow * 8;
  p.ufsrc = ws + WS_UF + (size_t)cb * 9 * 1024;
  p.sgsrc = ws + WS_ST2 + (rb * 256 + wid * 64) * 8;
  p.sgoff = (lane & 31) * 8;
  p.wdsrc = (const char*)Wd + (size_t)(xcd >> 1) * (1152 * 1152) + (idx * 4 + wid) * (81 * 128);
  const int G = lane >> 4;
  const unsigned rd0 = p.lds0 + (G & 1) * G1_UP + (8 * (G >> 1) + ((lane >> 2) & 3)) * 288 + 8 * (lane & 3);
  unsigned rd1 = rd0 + SLOT1, rd2 = rd0 + 2 * SLOT1;
  asm volatile("" : "+v"(rd1), "+v"(rd2));
  SCHED0;
  wait_vm<since<P1>(opc(1, 0, 8), 0, 0)>();
  __builtin_amdgcn_s_barrier();
  SCHED0;
  STAMPV(1);
  static_for<0, 9>([&](auto fc) { constexpr int f = decltype(fc)::value; st.wf[0][f] = g1_frag<0, f>(rd0); });
  SCHED0;
  g1_super<0>(st, p, rd0, rd1, 0, nullptr, 0, p.xbase + 4096);
  keep(st.pf[0]);
  PS1(2);
  g1_super<1>(st, p, rd1, rd2, 0, nullptr, 0, p.xbase + 2 * 4096);
  PS1(3);
#pragma unroll 1
  for (int s = 2; s < 6; ++s) {
    g1_super<2>(st, p, rd0 + (s & 3) * SLOT1, rd0 + ((s + 1) & 3) * SLOT1, 0, p.wbase + (size_t)(s + 3) * WSTEP1,
                p.wdst0 + ((s + 3) & 3) * SLOT1, p.xbase + (s + 1) * 4096);
  }
  unsigned rd3 = rd0 + 3 * SLOT1;
  asm volatile("" : "+v"(rd3));
  g1_super<6>(st, p, rd2, rd3, rd0, nullptr, 0, nullptr);
  PS1(4);
  {
    const int h = lane >> 5, ml = lane & 31;
    char* hbase = ws + WS_HF + (size_t)((rb * 8 + wid * 2) * 2) * 144 * 1024;
    const unsigned hoff = (ml >> 4) * (144 * 1024) + (h * 16 + (ml & 15)) * 16 + cb * 9 * 512;
    const unsigned ufrd = p.lds0 + G1_UF + p.xoff0;
    const char* wdsrc = (const char*)Wd + (size_t)(xcd >> 1) * (1152 * 1152) + (idx * 4 + wid) * 1024;
    const char* wdl = (const char*)Wd + (size_t)(xcd >> 1) * (1152 * 1152) + (1280 + ((idx * 4 + wid) & 15)) * 1024;
    wait_vm<2>();
#pragma unroll
    for (int i = 0; i < 8; ++i) { pinba(st.xt[i][0]); pinba(st.xt[i][1]); }
    pin2u(st.sg[0]); pin2u(st.sg[1]); keep(st.pf[1]);
#pragma unroll 1
    for (int spin = 0; spin < (1 << 22); ++spin) {
      if (__builtin_amdgcn_ballot_w64(st.sg[0][1] == 0u || st.sg[1][1] == 0u) == 0ull) break;
      __builtin_amdgcn_s_sleep(2);
      gload8_sc1<0>(st.sg[0], p.sgsrc, p.sgoff); gload8_sc1<256>(st.sg[1], p.sgsrc, p.sgoff);
      wait_vm<0>();
      pin2u(st.sg[0]); pin2u(st.sg[1]);
    }
    bf16x8 sb[2];
#pragma unroll
    for (int m = 0; m < 2; ++m) sb[m] = __builtin_bit_cast(bf16x8, (u32x4){st.sg[m][0], st.sg[m][1], st.sg[m][0], st.sg[m][1]});
    SCHED0;
    static_for<0, 10>([&](auto fc) {
      constexpr int f = decltype(fc)::value;
      constexpr int cur = f & 1, nxt = cur ^ 1;
      u32x4 o;
      float g0, g1, u0, u1;
      static_for<0, 18>([&](auto nc) {
        constexpr int n = decltype(nc)::value;
        constexpr int i = n >> 1, m = n & 1;
        if constexpr (f < 9) {
          if constexpr (n == 0) { wait_lgkm<7>(); SCHED0; }
          if constexpr (n == 10) { if constexpr (f < 8) wait_lgkm<10>(); else wait_lgkm<0>(); SCHED0; }
          if constexpr (i < 8) g1_mfma_ba<f>(st.acc[m][f], st.wf[cur][i], st.xt[i][m]);
          else g1_mfma<f, 0>(st.acc[m][f], st.wf[cur][i], sb[m]);
          if constexpr (f < 8 && m == 0) st.wf[nxt][i] = g1_lastfrag<i, f + 1>(rd3, rd0, ufrd);
          SCHED0;
        } else if constexpr (n == 0) {
          asm volatile("s_nop 15\n\ts_nop 7" ::: "memory");
          SCHED0;
        }
        if constexpr (f > 0 && n >= 1 && n <= 16) {
          constexpr int e = f - 1, u = (n - 1) >> 1, em = u >> 2, r = 2 * (u & 3);
          if constexpr (n == 1) g1_pin<e>(st.acc[0][e], st.acc[1][e]);
          if constexpr (n & 1) {
            g1_epa<e>(st.acc[em][e], r, g0, g1, u0, u1);
          } else {
            o[u & 3] = g1_epb<e>(g0, g1, u0, u1);
            if constexpr ((u & 3) == 3) hstore(hbase + em * 2 * 144 * 1024, hoff + e * 512, o);
          }
          if constexpr (f < 9) SCHED0;
        }
      });
    });
  }
  wait_vm<0>();
  keep(st.pf[2]); keep(st.pf[3]);
  PS1(5); STAMPOUT(0, 6, bid);
}

constexpr int SLOT2 = 36864;
constexpr int GEMM2_LDS = 4 * SLOT2;
struct P2 {
  static constexpr int plan(int s, int i, int c) {
    if (s == -1) return (i < 3 && c < 4) ? opc(2, i, c) : 0;
    if (s <= 5) return c == 9 ? opc(2, s + 3, i) : 0;
    return 0;
  }
  static constexpr int wait(int s, int i) { return since<P2>(opc(2, s, i), s, i); }
};
struct G2State {
  f32x4 acc[2][9];
  bf16x8 xr[12][2];
  bf16x8 wf[2][9];
};
struct G2Ptr {
  const char* xbase;
  unsigned xoff0, xoff1;
  unsigned rd[4];
};
template <int Z> DI void mfma16(f32x4& c, const bf16x8& a, const bf16x8& b) {
  if constexpr (Z) asm volatile("v_mfma_f32_16x16x32_bf16 %0, %1, %2, 0" : "=&v"(c) : "v"(a), "v"(b));
  else asm volatile("v_mfma_f32_16x16x32_bf16 %0, %1, %2, %0" : "+v"(c) : "v"(a), "v"(b));
}
template <int I, int F> DI bf16x8 g2_frag(unsigned rd) {
  return trfrag(rd + (32 * I) * 288 + 32 * F, rd + (32 * I + 8) * 288 + 32 * F);
}
constexpr size_t WSTEP2 = (size_t)128 * D_ * 2;
template <int OP> DI void g2_op(G2State& st, const G2Ptr& p) {
  constexpr int k = OP / 10000, t = (OP / 100) % 100, i = OP % 100;
  if constexpr (k == 2) {
    gload16<i * 1024>(st.xr[4 * (t % 3) + i][0], p.xbase + t * 4096, p.xoff0);
    gload16<i * 1024>(st.xr[4 * (t % 3) + i][1], p.xbase + t * 4096, p.xoff1);
  }
}
template <int I, int F, int H> DI s16x4 g2_half(unsigned rd) { return trhalf(rd + (32 * I + 8 * H) * 288 + 32 * F); }
template <int S>
DI void g2_super(G2State& st, const G2Ptr& p) {
  constexpr int R = S % 3;
  static_for<0, 4>([&](auto ic) {
    constexpr int i = decltype(ic)::value;
    constexpr int cur = i & 1, nxt = cur ^ 1;
    wait_vm<P2::wait(S, i)>();
    pinb(st.xr[4 * R + i][0]); pinb(st.xr[4 * R + i][1]);
    if constexpr (i == 3) {
      asm volatile("" ::: "memory");
      __builtin_amdgcn_s_barrier();
      asm volatile("" ::: "memory");
    }
    SCHED0;
    static_for<0, 9>([&](auto fc) {
      constexpr int f = decltype(fc)::value;
      constexpr bool ld = (i < 3) || (S < 7) || (f >= 5);
      constexpr int li = (i < 3) ? i + 1 : (S < 7) ? 0 : (f >= 5 ? f - 5 : 0), lf = (i == 3 && S == 7) ? 8 : f;
      constexpr int ls = (i < 3) ? (S & 3) : ((S + 1) & 3), ld_to = (i == 3 && S == 7) ? li : f;
      if constexpr (f == 0) { wait_lgkm<8>(); SCHED0; }
      if constexpr (f == 5) { wait_lgkm<(i == 3 && S == 7) ? 0 : 10>(); SCHED0; }
      s16x4 lo;
      mfma16<(S == 0 && i == 0)>(st.acc[0][f], st.wf[cur][f], st.xr[4 * R + i][0]);
      if constexpr (ld) lo = g2_half<li, lf, 0>(p.rd[ls]);
      SCHED0;
      mfma16<(S == 0 && i == 0)>(st.acc[1][f], st.wf[cur][f], st.xr[4 * R + i][1]);
      if constexpr (ld) st.wf[nxt][ld_to] = join(lo, g2_half<li, lf, 1>(p.rd[ls]));
      SCHED0;
    });
    g2_op<P2::plan(S, i, 9)>(st, p);
    SCHED0;
  });
}
DI u32x4 g2_pack(const f32x4& a, const f32x4& b) {
  return (u32x4){pack2(a[0], a[1]), pack2(a[2], a[3]), pack2(b[0], b[1]), pack2(b[2], b[3])};
}
DI f32x4 g2_unpack(unsigned a, unsigned b) { return (f32x4){bflo(a), bfhi(a), bflo(b), bfhi(b)}; }
DI void g2_out(f32x4 s, float is2, const u32x2& x1, const f32x4& gb, u16* out, unsigned oo) {
  s = s * is2;
  float o0 = bflo(x1[0]) + rbf(gb[0] * rbf(s[0])), o1 = bfhi(x1[0]) + rbf(gb[1] * rbf(s[1]));
  float o2 = bflo(x1[1]) + rbf(gb[2] * rbf(s[2])), o3 = bfhi(x1[1]) + rbf(gb[3] * rbf(s[3]));
  u32x2 o = {pack2(o0, o1), pack2(o2, o3)};
  gstore8((char*)out, oo, o);
}
extern "C" __global__ __launch_bounds__(512, 1)
void k_gemm2(const u16* __restrict__ Wd, char* __restrict__ ws, u16* __restrict__ out) {
  extern __shared__ __attribute__((aligned(16))) char smem[];
  const int tid = threadIdx.x, lane = tid & 63;
  const int wid8 = __builtin_amdgcn_readfirstlane(tid >> 6);
  const int wid = wid8 & 3;
  const int bid = blockIdx.x;
  const int xcd = bid & 7, idx = bid >> 3;
  const int q = xcd >> 1;
  const int rb = (xcd & 1) * 8 + (idx >> 2);
  const int db = idx & 3;
  const unsigned lds0 = (unsigned)(size_t)(lds_cp)smem;
  if (wid8 >= 4) {
    const char* wsrc = (const char*)Wd + (size_t)(q * 1152 + 32 * wid) * (D_ * 2) + db * 288;
    unsigned voff[9];
#pragma unroll
    for (int i = 0; i < 9; ++i) {
      int c = 64 * i + lane, j = c % 18;
      voff[i] = (c / 18) * (D_ * 2) + (j < 16 ? ((j + 4 * q + 4) & 15) : j) * 16;
    }
    const unsigned wdst0 = lds0 + wid * 9216;
    unsigned pf;
    DMA9(wsrc, wdst0);
    CODEPF(pf, "k_gemm2", "k_zend", wid, lane);
    DMA9(wsrc + WSTEP2, wdst0 + SLOT2);
    wait_vm<9>();
    keep(pf);
    __builtin_amdgcn_s_barrier();
    DMA9(wsrc + 2 * WSTEP2, wdst0 + 2 * SLOT2);
    DMA9(wsrc + 3 * WSTEP2, wdst0 + 3 * SLOT2);
    wait_vm<18>();
    __builtin_amdgcn_s_barrier();
#pragma unroll 1
    for (int s = 1; s < 6; ++s) {
      wait_vm<9>();
      __builtin_amdgcn_s_barrier();
      DMA9(wsrc + (s + 3) * WSTEP2, wdst0 + ((s + 3) & 3) * SLOT2);
    }
    wait_vm<9>();
    __builtin_amdgcn_s_barrier();
    wait_vm<0>();
    __builtin_amdgcn_s_barrier();
    return;
  }
  STAMPDECL STAMPV(0);
  G2Ptr p;
  p.xbase = ws + WS_HF + ((size_t)(rb * 8 + wid * 2) * 144 + q * 36) * 1024;
  p.xoff0 = lane * 16; p.xoff1 = lane * 16 + 144 * 1024;
  G2State st;
  static_for<0, 3>([&](auto ic) {
    static_for<0, 4>([&](auto cc) { g2_op<P2::plan(-1, decltype(ic)::value, decltype(cc)::value)>(st, p); });
  });
  SCHED0;
  const int g = lane >> 4, ml = lane & 15;
  const unsigned rd0 = lds0 + (16 * (g >> 1) + 4 * (g & 1) + ((lane >> 2) & 3)) * 288 + 8 * (lane & 3);
#pragma unroll
  for (int i = 0; i < 4; ++i) p.rd[i] = rd0 + i * SLOT2;
  asm volatile("" : "+v"(p.rd[0]), "+v"(p.rd[1]), "+v"(p.rd[2]), "+v"(p.rd[3]));
  const int tw = (rb * 4 + db) * 4 + wid;
  char* pb = ws + WS_PART + (size_t)tw * 9 * 4096;
  char* fb = ws + WS_FLAG + (size_t)tw * 256;
  const char* x1b = ws + WS_X1F + (size_t)(rb * 4 + wid) * 36 * 1024 + db * 9 * 1024;
  unsigned x1o = (g >> 1) * 512 + ml * 16 + (g & 1) * 8;
  unsigned gbo = (db * 144 + 4 * g) * 4;
  unsigned sgo = WS_ST2 + (rb * 128 + wid * 32 + ml) * 8 + 4;
  unsigned ob = ((rb * 128 + wid * 32 + ml) * D_ + db * 144 + 4 * g) * 2;
  pb = gpin(pb); fb = gpin(fb); x1b = gpin(x1b);
  asm volatile("" : "+v"(x1o), "+v"(gbo), "+v"(sgo), "+v"(ob));
  SCHED0;
  __builtin_amdgcn_s_barrier();
  SCHED0;
  STAMPV(1);
  static_for<0, 9>([&](auto fc) { constexpr int f = decltype(fc)::value; st.wf[0][f] = g2_frag<0, f>(p.rd[0]); });
  SCHED0;
  static_for<0, 8>([&](auto sc) { g2_super<decltype(sc)::value>(st, p); });
  const unsigned lo = lane * 16;
  wait_vm<0>();
#pragma unroll
  for (int i = 0; i < 4; ++i) { pinb(st.xr[8 + i][0]); pinb(st.xr[8 + i][1]); }
  SCHED0;
  u32x2 x1[5]; f32x4 gb[3]; unsigned sg[2];
  static_for<0, 9>([&](auto nc) {
    constexpr int n = decltype(nc)::value;
    constexpr int r = (n == 0) ? 8 : n - 1, rp = (n == 1) ? 8 : n - 2;
    constexpr int cur = n & 1, nxt = cur ^ 1;
    static_for<0, 4>([&](auto ic) {
      constexpr int i = decltype(ic)::value;
      s16x4 hl;
      mfma16<0>(st.acc[0][r], st.wf[cur][i], st.xr[8 + i][0]);
      if constexpr (n < 8) hl = g2_half<i, n, 0>(p.rd[0]);
      SCHED0;
      mfma16<0>(st.acc[1][r], st.wf[cur][i], st.xr[8 + i][1]);
      if constexpr (n < 8) st.wf[nxt][i] = join(hl, g2_half<i, n, 1>(p.rd[0]));
      SCHED0;
      if constexpr (n >= 1 && n <= 7 && i == 1) {
        pin4(st.acc[0][rp]); pin4(st.acc[1][rp]);
        const int dp = (rp == 8) ? 8 : ((2 * q + 2 + rp) & 7);
        gstore16_sc1<0>(pb + dp * 4096 + q * 1024, lo, g2_pack(st.acc[0][rp], st.acc[1][rp]));
        SCHED0;
      }
      if constexpr (n == 0) {
        if constexpr (i < 2) {
          x1[2 * i] = *(const u32x2*)(x1b + (2 * q + i) * 1024 + x1o);
          x1[2 * i + 1] = *(const u32x2*)(x1b + (2 * q + i) * 1024 + 256 + x1o);
          gb[i] = *(const f32x4*)(ws + WS_GB + (2 * q + i) * 64 + gbo);
        } else if constexpr (i == 2) {
          x1[4] = *(const u32x2*)(x1b + 8 * 1024 + (q & 1) * 256 + x1o);
          gb[2] = *(const f32x4*)(ws + WS_GB + 8 * 64 + gbo);
        } else {
          sg[0] = *(const unsigned*)(ws + sgo);
          sg[1] = *(const unsigned*)(ws + 128 + sgo);
        }
        SCHED0;
      }
    });
  });
  STAMPV(2);
  wait_vm<0>();
  STAMPV(3);
#pragma unroll
  for (int t = 0; t < 5; ++t) pin2u(x1[t]);
#pragma unroll
  for (int t = 0; t < 3; ++t) pin4(gb[t]);
  asm volatile("" : "+v"(sg[0]), "+v"(sg[1]));
  if (lane < 4) asm volatile("global_store_dword %0, %1, %2 sc1" ::"v"(lane * 64), "v"(1), "s"(fb + q * 4) : "memory");
  float is2[2];
#pragma unroll
  for (int m = 0; m < 2; ++m) { float s = bfhi(sg[m]); is2[m] = 1.f / (s * s); }
  asm volatile("" : "+v"(is2[0]), "+v"(is2[1]));
#pragma unroll
  for (int t = 6; t < 8; ++t) { pin4(st.acc[0][t]); pin4(st.acc[1][t]); }
  SCHED0;
#pragma unroll 1
  for (int spin = 0; spin < (1 << 22); ++spin) {
    u32x4 u;
    gload16_sc1<0>(u, fb + q * 64, 0u);
    wait_vm<0>();
    pin4u(u);
    if (__builtin_amdgcn_readfirstlane(u[0] & u[1] & u[2] & u[3]) != 0) break;
    __builtin_amdgcn_s_sleep(1);
  }
  STAMPV(4);
  u32x4 pr[2][3]; u32x2 p8[3];
#pragma unroll
  for (int k = 0; k < 3; ++k) {
    const int qs = (q + 1 + k) & 3;
    gload16_sc1<0>(pr[0][k], pb + (2 * q) * 4096 + qs * 1024, lo);
    gload16_sc1<0>(pr[1][k], pb + (2 * q + 1) * 4096 + qs * 1024, lo);
    gload8_sc1<0>(p8[k], pb + 8 * 4096 + qs * 1024 + (q & 1) * 8, lo);
  }
  wait_vm<0>();
#pragma unroll
  for (int k = 0; k < 3; ++k) { pin4u(pr[0][k]); pin4u(pr[1][k]); pin2u(p8[k]); }
#pragma unroll
  for (int t = 0; t < 4; ++t) {
    const int d = t >> 1, m = t & 1;
    f32x4 v = (st.acc[m][6 + d] + g2_unpack(pr[d][0][2 * m], pr[d][0][2 * m + 1])) +
              (g2_unpack(pr[d][1][2 * m], pr[d][1][2 * m + 1]) + g2_unpack(pr[d][2][2 * m], pr[d][2][2 * m + 1]));
    g2_out(v, is2[m], x1[t], gb[d], out, ob + m * 16 * D_ * 2 + (2 * q + d) * 32);
  }
  if (q < 2) {
    f32x4 own = q ? st.acc[1][8] : st.acc[0][8];
    f32x4 v = (own + g2_unpack(p8[0][0], p8[0][1])) + (g2_unpack(p8[1][0], p8[1][1]) + g2_unpack(p8[2][0], p8[2][1]));
    g2_out(v, q ? is2[1] : is2[0], x1[4], gb[2], out, ob + q * 16 * D_ * 2 + 8 * 32);
  }
  STAMPDRAIN; STAMPV(5); STAMPOUT(1, 6, bid);
}

extern "C" __global__ void k_zend() {}

extern "C" void kernel_launch(void* const* d_in, const int* in_sizes, int n_in, void* d_out, int out_size,
                              void* d_ws, size_t ws_size, hipStream_t stream) {
  const u16* Aq = (const u16*)d_in[0];
  const u16* Ak = (const u16*)d_in[1];
  const u16* Av = (const u16*)d_in[2];
  const u16* shk = (const u16*)d_in[3];
  const u16* ssmk = (const u16*)d_in[4];
  const u16* ssmD = (const u16*)d_in[5];
  const u16* Wg = (const u16*)d_in[7];
  const u16* Wu = (const u16*)d_in[8];
  const u16* Wd = (const u16*)d_in[9];
  const u16* cvec = (const u16*)d_in[10];
  const u16* wmod = (const u16*)d_in[11];
  char* ws = (char*)d_ws;
  (void)hipFuncSetAttribute((const void*)k_conv, hipFuncAttributeMaxDynamicSharedMemorySize, CONV_LDS);
  (void)hipFuncSetAttribute((const void*)k_gemm1, hipFuncAttributeMaxDynamicSharedMemorySize, GEMM1_LDS);
  (void)hipFuncSetAttribute((const void*)k_gemm2, hipFuncAttributeMaxDynamicSharedMemorySize, GEMM2_LDS);
  k_pre<<<NMOD + NTILE + NSTAT, 256, 0, stream>>>(Aq, Ak, Av, shk, cvec, wmod, ws);
  k_conv<<<NCONV + NUV + 1, 512, CONV_LDS, stream>>>(ssmk, ssmD, Wg, Wu, Wd, ws);
  k_gemm1<<<256, 256, GEMM1_LDS, stream>>>(Wg, Wu, Wd, ws);
  k_gemm2<<<256, 512, GEMM2_LDS, stream>>>(Wd, ws, (u16*)d_out);
}
```

```cpp
#include <hip/hip_runtime.h>
#include <hip/hip_bf16.h>
#include <stdint.h>
#include <type_traits>

typedef __attribute__((ext_vector_type(8))) short bf16x8;
typedef __attribute__((ext_vector_type(4))) short s16x4;
typedef __attribute__((ext_vector_type(4))) float f32x4;
typedef __attribute__((ext_vector_type(2))) float f32x2;
typedef __attribute__((ext_vector_type(16))) float f32x16;
typedef __attribute__((ext_vector_type(4))) unsigned u32x4;
typedef __attribute__((ext_vector_type(2))) unsigned u32x2;
typedef __attribute__((ext_vector_type(2))) __bf16 bf16x2_t;
typedef __attribute__((address_space(3))) s16x4* lds_s16x4p;
typedef __attribute__((address_space(3))) char* lds_cp;
typedef unsigned short u16;

#define DI __device__ __forceinline__

constexpr int S_ = 2048, D_ = 576, F_ = 4608;

constexpr size_t SD2 = (size_t)S_ * D_ * 2;
constexpr size_t WS_GB   = 0;
constexpr size_t WS_ST1  = 16384;
constexpr size_t WS_ST2  = 32768;
constexpr size_t WS_FLAG = 49152;
constexpr size_t WS_MODP = WS_FLAG + 65536;
constexpr size_t WS_UF   = WS_MODP + 49152;
constexpr size_t WS_KVR  = WS_UF + 288 * 1024;
constexpr size_t WS_QG   = WS_KVR + SD2;
constexpr size_t WS_XF   = WS_QG + SD2;
constexpr size_t WS_X1F  = WS_XF + SD2;
constexpr size_t WS_HF   = WS_X1F + SD2;
constexpr size_t WS_PART = WS_HF + (size_t)S_ * F_ * 2;

DI float bf2f(unsigned u) { return __uint_as_float(u << 16); }
DI float bflo(unsigned u) { return __uint_as_float(u << 16); }
DI float bfhi(unsigned u) { return __uint_as_float(u & 0xffff0000u); }
DI unsigned pack2(float a, float b) {
  f32x2 v = {a, b};
  bf16x2_t r = __builtin_convertvector(v, bf16x2_t);
  return __builtin_bit_cast(unsigned, r);
}
DI float rbf(float a) { return bflo(pack2(a, 0.f)); }
DI float dot2(unsigned a, unsigned b, float c) {
  return __builtin_amdgcn_fdot2_f32_bf16(__builtin_bit_cast(bf16x2_t, a), __builtin_bit_cast(bf16x2_t, b), c, false);
}
DI u32x2 trread(unsigned addr) {
  return __builtin_bit_cast(u32x2, __builtin_amdgcn_ds_read_tr16_b64_v4i16((lds_s16x4p)(size_t)addr));
}
template <int I, int N, typename Fn> DI void static_for(Fn&& fn) {
  if constexpr (I < N) { fn(std::integral_constant<int, I>{}); static_for<I + 1, N>(fn); }
}
#define SCHED0 __builtin_amdgcn_sched_barrier(0)
template <typename T> DI T* gpin(T* p) {
  unsigned long long v = (unsigned long long)p;
  asm volatile("" : "+s"(v));
  return (T*)(__attribute__((address_space(1))) T*)v;
}
template <int N> DI void wait_vm() { asm volatile("s_waitcnt vmcnt(%0)" ::"i"(N) : "memory"); }
template <int OFF> DI void dma1k(const char* sbase, unsigned voff, unsigned ldsdst) {
  asm volatile("s_add_u32 m0, %2, %3\n\ts_nop 0\n\tglobal_load_lds_dwordx4 %0, %1"
               ::"v"(voff), "s"(sbase), "s"(ldsdst), "i"(OFF) : "memory", "scc");
}

#define SYMADDR(V, SYM) asm volatile("s_getpc_b64 vcc\n\ts_add_u32 vcc_lo, vcc_lo, " SYM "@rel32@lo+4\n\t" \
    "s_addc_u32 vcc_hi, vcc_hi, " SYM "@rel32@hi+12\n\ts_mov_b64 %0, vcc" : "=s"(V) :: "vcc", "scc")
#define CODEPF(DST, SYM, END, CHUNK, LANE) do { unsigned long long a_, e_; SYMADDR(a_, SYM); SYMADDR(e_, END); \
    unsigned n_ = (e_ > a_) ? (unsigned)(e_ - a_) - 4u : 0u; unsigned o_ = (unsigned)(CHUNK) * 8192u + (unsigned)(LANE) * 128u; \
    o_ = o_ < n_ ? o_ : n_; \
    asm volatile("global_load_dword %0, %1, %2" : "=v"(DST) : "v"(o_), "s"(a_) : "memory"); } while (0)
#define SCP ""
#define SCQ "sc1"
#define SCC ""
#define SCO "sc1"
DI void gstore8(char* sbase, unsigned voff, const u32x2& v) {
  asm volatile("global_store_dwordx2 %0, %1, %2 " SCO ::"v"(voff), "v"(v), "s"(sbase) : "memory");
}
DI void gstore8c(char* sbase, unsigned voff, const u32x2& v) {
  asm volatile("global_store_dwordx2 %0, %1, %2 " SCC ::"v"(voff), "v"(v), "s"(sbase) : "memory");
}
DI void st16(void* p, const u32x4& v) { asm volatile("global_store_dwordx4 %0, %1, off " SCP "\n\ts_nop 1" ::"v"(p), "v"(v) : "memory"); }
DI void st16q(void* p, const u32x4& v) { asm volatile("global_store_dwordx4 %0, %1, off " SCQ "\n\ts_nop 1" ::"v"(p), "v"(v) : "memory"); }
DI void st8(void* p, const u32x2& v) { asm volatile("global_store_dwordx2 %0, %1, off " SCP ::"v"(p), "v"(v) : "memory"); }
DI void st4(void* p, unsigned v) { asm volatile("global_store_dword %0, %1, off " SCP ::"v"(p), "v"(v) : "memory"); }
DI void keep(unsigned v) { asm volatile("" ::"v"(v)); }

#define STAMPV(i)
#define STAMPDECL
#define PS(i)
#define STAMPARG
#define STAMPPASS
#define STAMPDRAIN
#define STAMPOUT(K, N, B)

constexpr int NTILE = 288, NSTAT = 128, NMOD = 162;
constexpr int TRS = 160;
extern "C" __global__ __launch_bounds__(256)
void k_pre(const u16* __restrict__ Aq, const u16* __restrict__ Ak, const u16* __restrict__ Av,
           const u16* __restrict__ shk, const u16* __restrict__ cvec, const u16* __restrict__ wmod,
           const u16* __restrict__ ssmk, char* __restrict__ ws) {
  __shared__ __attribute__((aligned(16))) char smem[64 * TRS];
  const int tid = threadIdx.x, lane = tid & 63, wid = tid >> 6;
  const int bid = blockIdx.x;
  unsigned pf;
  CODEPF(pf, "k_pre", "k_gemm1", wid, lane);
  Aq = gpin(Aq); Ak = gpin(Ak); Av = gpin(Av); shk = gpin(shk); cvec = gpin(cvec); wmod = gpin(wmod); ws = gpin(ws);
  if (bid < NTILE) {
    STAMPDECL STAMPV(0);
    const int tb = bid;
    const int st = tb / 9, dt = tb % 9;
    const int r2 = tid >> 3, c8 = tid & 7;
    const int sA = st * 64 + 2 * r2;
    const int ch = dt * 64 + c8 * 8;
    u32x4 kr[5], vr[2], qr[2], wr[4];
#pragma unroll
    for (int i = 0; i < 5; ++i) {
      int s = sA - 3 + i;
      kr[i] = *(const u32x4*)(Ak + (size_t)(s < 0 ? 0 : s) * D_ + ch);
    }
#pragma unroll
    for (int i = 0; i < 2; ++i) vr[i] = *(const u32x4*)(Av + (size_t)(sA + i) * D_ + ch);
#pragma unroll
    for (int i = 0; i < 4; ++i) wr[i] = *(const u32x4*)(shk + ch * 4 + i * 8);
#pragma unroll
    for (int i = 0; i < 2; ++i) qr[i] = *(const u32x4*)(Aq + (size_t)(sA + i) * D_ + ch);
    SCHED0;
#pragma unroll
    for (int i = 0; i < 3; ++i)
      if (sA - 3 + i < 0) kr[i] = (u32x4){0u, 0u, 0u, 0u};
#pragma unroll
    for (int i = 0; i < 2; ++i) {
      u32x4 o;
#pragma unroll
      for (int p = 0; p < 4; ++p) {
        float a0 = bflo(kr[i][p]) * bfhi(wr[p][1]);
        a0 = fmaf(bflo(kr[i + 1][p]), bflo(wr[p][1]), a0);
        a0 = fmaf(bflo(kr[i + 2][p]), bfhi(wr[p][0]), a0);
        a0 = fmaf(bflo(kr[i + 3][p]), bflo(wr[p][0]), a0);
        float a1 = bfhi(kr[i][p]) * bfhi(wr[p][3]);
        a1 = fmaf(bfhi(kr[i + 1][p]), bflo(wr[p][3]), a1);
        a1 = fmaf(bfhi(kr[i + 2][p]), bfhi(wr[p][2]), a1);
        a1 = fmaf(bfhi(kr[i + 3][p]), bflo(wr[p][2]), a1);
        unsigned ks = pack2(a0, a1);
        o[p] = pack2(bflo(ks) * bflo(vr[i][p]), bfhi(ks) * bfhi(vr[i][p]));
      }
      *(u32x4*)(smem + (63 - (2 * r2 + i)) * TRS + c8 * 16) = o;
    }
    {
      char* qo = ws + WS_QG + (size_t)(ch >> 2) * (S_ * 8) + sA * 8;
      st16q(qo, (u32x4){qr[0][0], qr[0][1], qr[1][0], qr[1][1]});
      st16q(qo + S_ * 8, (u32x4){qr[0][2], qr[0][3], qr[1][2], qr[1][3]});
    }
    __syncthreads();
    STAMPV(1);
    {
      const int g = lane >> 4, q = (lane >> 2) & 3, p = lane & 3;
      const unsigned base = (unsigned)(size_t)(lds_cp)smem + (8 * g + q) * TRS + wid * 32 + p * 8;
      const int d = dt * 64 + wid * 16 + (lane & 15);
      u16* kvo = (u16*)(ws + WS_KVR) + (size_t)d * S_ + (1984 - 64 * st) + 8 * g;
#pragma unroll
      for (int hf = 0; hf < 2; ++hf) {
        u32x2 a = trread(base + hf * 32 * TRS), b = trread(base + (hf * 32 + 4) * TRS);
        st16(kvo + hf * 32, (u32x4){a[0], a[1], b[0], b[1]});
      }
    }
    STAMPV(2); STAMPDRAIN; STAMPV(3); STAMPOUT(3, 4, tb);
  } else if (bid < NTILE + NMOD) {
    STAMPDECL STAMPV(0);
    const int nb = (bid - NTILE) % 54, kp = (bid - NTILE) / 54;
    const int cg = tid & 7, rg = kp * 192 + (tid >> 3);
    u32x4 wv[6];
    u16 cr[6];
#pragma unroll
    for (int i = 0; i < 6; ++i) wv[i] = *(const u32x4*)(wmod + (size_t)(rg + 32 * i) * 3456 + nb * 64 + cg * 8);
#pragma unroll
    for (int i = 0; i < 6; ++i) cr[i] = cvec[rg + 32 * i];
    SCHED0;
    float a[8];
#pragma unroll
    for (int j = 0; j < 8; ++j) a[j] = 0.f;
#pragma unroll
    for (int i = 0; i < 6; ++i) {
      float cv = bf2f(cr[i]);
      float sv = rbf(cv / (1.f + __expf(-cv)));
#pragma unroll
      for (int p = 0; p < 4; ++p) { a[2 * p] += sv * bflo(wv[i][p]); a[2 * p + 1] += sv * bfhi(wv[i][p]); }
    }
    float* red = (float*)smem;
    *(f32x4*)(red + (tid >> 3) * 68 + cg * 8) = (f32x4){a[0], a[1], a[2], a[3]};
    *(f32x4*)(red + (tid >> 3) * 68 + cg * 8 + 4) = (f32x4){a[4], a[5], a[6], a[7]};
    __syncthreads();
    {
      const int col = tid >> 2, part = tid & 3;
      float r = 0.f;
#pragma unroll
      for (int i = 0; i < 8; ++i) r += red[(part * 8 + i) * 68 + col];
      r += __shfl_xor(r, 1); r += __shfl_xor(r, 2);
      if (part == 0) st4((float*)(ws + WS_MODP) + kp * 3456 + nb * 64 + col, __float_as_uint(r));
    }
    STAMPV(1); STAMPOUT(5, 2, bid - NTILE);
  } else {
    const int sb = bid - NMOD - NTILE;
    if (wid < 2) {
      const int row = sb * 16 + wid * 8 + (lane >> 3), c = lane & 7;
      u32x4 v[9];
#pragma unroll
      for (int i = 0; i < 9; ++i) v[i] = *(const u32x4*)(Aq + (size_t)row * D_ + (c + 8 * i) * 8);
      float s1 = 0.f;
#pragma unroll
      for (int i = 0; i < 9; ++i)
#pragma unroll
        for (int p = 0; p < 4; ++p) s1 = dot2(v[i][p], 0x3f803f80u, s1);
      s1 += __shfl_xor(s1, 1); s1 += __shfl_xor(s1, 2); s1 += __shfl_xor(s1, 4);
      const float mean = s1 * (1.f / D_);
      float s2 = 0.f;
#pragma unroll
      for (int i = 0; i < 9; ++i)
#pragma unroll
        for (int p = 0; p < 4; ++p) {
          float a = bflo(v[i][p]) - mean, b = bfhi(v[i][p]) - mean;
          s2 = fmaf(a, a, s2); s2 = fmaf(b, b, s2);
        }
      s2 += __shfl_xor(s2, 1); s2 += __shfl_xor(s2, 2); s2 += __shfl_xor(s2, 4);
      if (c == 0) {
        float sd = rbf(sqrtf(rbf(rbf(s2 * (1.f / D_)) + 1e-5f)));
        const float rs = 1.f / sd;
        st8(ws + WS_ST1 + row * 8, (u32x2){__float_as_uint(rs), __float_as_uint(-rbf(mean) * rs)});
      }
    } else {
      if (wid == 3 && lane < 8) st16(ws + WS_ST2 + sb * 128 + lane * 16, (u32x4){0u, 0u, 0u, 0u});
      wait_vm<0>();
    }
  }
  keep(pf);
}

constexpr int NCONV = 144, NUV = 108;
#define EPL 1
#define WPF 64
#define CVO 4
constexpr int KCS = 4160;
constexpr int KER_BYTES = 4 * KCS;
constexpr int RS = 4608;
constexpr int YS = 4096 + 64;
constexpr int CB_R = 4 * KER_BYTES;
constexpr int CB_Y = CB_R + 4 * RS;
constexpr int CB_X = CB_Y + 4 * YS;
constexpr int CONV_LDS = CB_X + 32768;
DI float modv(const char* ws, int n) {
  const float* m = (const float*)(ws + WS_MODP);
  return rbf((m[n] + m[3456 + n]) + m[2 * 3456 + n]);
}
constexpr int tri_start(int u) { return 8 * u - u * (u - 1) / 2; }
constexpr int tri_row(int n) { int u = 0; while (tri_start(u + 1) <= n) ++u; return u; }

extern "C" __global__ __launch_bounds__(512, 1)
void k_conv(const u16* __restrict__ ssmk, const u16* __restrict__ ssmD, const u16* __restrict__ Wg,
            const u16* __restrict__ Wu, const u16* __restrict__ Wd, char* __restrict__ ws) {
  extern __shared__ __attribute__((aligned(16))) char smem[];
  const int tid = threadIdx.x, lane = tid & 63;
  const int wid = __builtin_amdgcn_readfirstlane(tid >> 6);
  const int bid = blockIdx.x;
  unsigned pf;
  CODEPF(pf, "k_conv", "k_gemm2", wid, lane);
  ssmk = gpin(ssmk); ssmD = gpin(ssmD); Wg = gpin(Wg); Wu = gpin(Wu); ws = gpin(ws);
  if (bid < NCONV) {
    STAMPDECL STAMPV(0);
    const unsigned lds0 = (unsigned)(size_t)(lds_cp)smem;
    const int grp = (bid & 7) * 18 + (bid >> 3);
    const int c0 = grp * 4;
    const int cw = wid & 3, hw = wid >> 2;
    f32x2 st1[4];
    u32x2 q4[4];
    float mp[3];
    float Dv;
    auto epload = [&]() {
      const int mj = lane >> 2;
      const float* m = (const float*)(ws + WS_MODP) + (mj < 3 ? mj : mj == 3 ? 2 : 4) * D_ + c0 + (lane & 3);
#pragma unroll
      for (int k = 0; k < 3; ++k) mp[k] = m[k * 3456];
#pragma unroll
      for (int i = 0; i < 4; ++i) {
        st1[i] = *(const f32x2*)(ws + WS_ST1 + (size_t)(512 * i + tid) * 8);
        q4[i] = *(const u32x2*)(ws + WS_QG + (size_t)grp * (S_ * 8) + (512 * i + tid) * 8);
      }
      SCHED0;
    };
    {
      const u16* kerg = ssmk + (size_t)(c0 + cw) * S_;
      char* kb = smem + cw * KER_BYTES;
      u32x4 a[2]; u32x2 b[2];
#pragma unroll
      for (int i = 0; i < 2; ++i) {
        int ch = (hw * 2 + i) * 64 + lane;
        a[i] = *(const u32x4*)(kerg + ch * 8);
        b[i] = *(const u32x2*)(kerg + (ch < 255 ? ch * 8 + 8 : 0));
      }
      SCHED0;
      {
        const char* kvsrc = ws + WS_KVR + (size_t)(c0 + cw) * 4096 + hw * 2048;
        const unsigned vo = lane * 16;
        dma1k<0>(kvsrc, vo, lds0 + CB_R + cw * RS + hw * 2048);
        dma1k<1024>(kvsrc, vo + 1024, lds0 + CB_R + cw * RS + hw * 2048);
      }
      Dv = bf2f(ssmD[c0 + cw]);
      SCHED0;
      if (hw == 1 && lane == 63) b[1] = (u32x2){0u, 0u};
#pragma unroll
      for (int i = 0; i < 2; ++i) {
        int ch = (hw * 2 + i) * 64 + lane;
        u32x4 c1, c2, c3;
        c1[0] = __builtin_amdgcn_alignbit(a[i][1], a[i][0], 16); c1[1] = __builtin_amdgcn_alignbit(a[i][2], a[i][1], 16);
        c1[2] = __builtin_amdgcn_alignbit(a[i][3], a[i][2], 16); c1[3] = __builtin_amdgcn_alignbit(b[i][0], a[i][3], 16);
        c2[0] = a[i][1]; c2[1] = a[i][2]; c2[2] = a[i][3]; c2[3] = b[i][0];
        c3[0] = c1[1]; c3[1] = c1[2]; c3[2] = c1[3]; c3[3] = __builtin_amdgcn_alignbit(b[i][1], b[i][0], 16);
        int off = (ch * 8 + 16) * 2;
        *(u32x4*)(kb + off) = a[i];
        *(u32x4*)(kb + KCS + off) = c1;
        *(u32x4*)(kb + 2 * KCS + off) = c2;
        *(u32x4*)(kb + 3 * KCS + off) = c3;
        if (ch == 0) {
          u32x4 z = {0u, 0u, 0u, 0u};
          *(u32x4*)(kb) = z; *(u32x4*)(kb + KCS) = z; *(u32x4*)(kb + 2 * KCS) = z; *(u32x4*)(kb + 3 * KCS) = z;
          u32x4 e1 = z, e2 = z, e3 = z;
          e1[3] = a[i][0] << 16;
          e2[3] = a[i][0];
          e3[2] = a[i][0] << 16; e3[3] = c1[0];
          *(u32x4*)(kb + 16) = z;
          *(u32x4*)(kb + KCS + 16) = e1;
          *(u32x4*)(kb + 2 * KCS + 16) = e2;
          *(u32x4*)(kb + 3 * KCS + 16) = e3;
        }
      }
      if (hw == 1 && lane < 32) *(u32x4*)(smem + CB_R + cw * RS + 4096 + lane * 16) = (u32x4){0u, 0u, 0u, 0u};
    }
    wait_vm<(EPL == 0) ? 11 : 0>();
    __builtin_amdgcn_s_waitcnt(0xC07F);
    __builtin_amdgcn_s_barrier();
    SCHED0;
    STAMPV(1);
    epload();
    float xf[4][4], hh[4][4], cA[4], cB[4], cb1[4], mv, mt;
    u32x2 kv[4];
    unsigned wm = 0, wm2 = 0;
    const int wl = (bid >> 3) * WPF + ((tid & 255) < WPF ? (tid & 255) : 0);
    const unsigned wo2 = (unsigned)((wl / 18) * (F_ * 2) + ((bid & 7) >> 1) * 2304 + (wl % 18) * 128);
    const unsigned wo = (unsigned)(bid * 288 + (tid < 287 ? tid : 287)) * 128u;
    f32x4 acc[8];
#pragma unroll
    for (int t = 0; t < 8; ++t) acc[t] = (f32x4){0.f, 0.f, 0.f, 0.f};
    const int b = lane & 15, g = lane >> 4;
    {
      typedef __attribute__((address_space(3))) const u32x2* lp2;
      typedef __attribute__((address_space(3))) const bf16x8* lp4;
      const int x0 = 1 + b + 8 * g;
      unsigned a_lo = lds0 + cw * KER_BYTES + (x0 & 3) * KCS + (x0 & ~3) * 2 + hw * 256, a_hi = a_lo + 8;
      unsigned gb = lds0 + CB_R + cw * RS + (2032 - 16 * b + 8 * g) * 2 - 7 * 512 + hw * 256;
      asm volatile("" : "+v"(a_lo), "+v"(a_hi), "+v"(gb));
      bf16x8 G[2][8];
      u32x2 Al[2][8], Ah[2][8];
      const unsigned kvo = lds0 + CB_R + cw * RS + (2044 - 1024 * hw - 16 * b - 4 * g) * 2;
      const float one = ((lane >> 2) == 1 || lane >= 16) ? 1.f : 0.f;
      auto ld = [&](auto uc, auto jc) {
        constexpr int u0 = decltype(uc)::value, j = decltype(jc)::value, bf = u0 & 1, i = j < 2 ? 0 : (j - 8) >> 1;
        if constexpr (j >= 2 && j < 10) G[bf][j - 2] = *(lp4)(size_t)(gb + u0 * 64 + (9 - j) * 512);
        else if constexpr ((j & 1) == 0) Al[bf][i] = *(lp2)(size_t)(a_lo + u0 * 64 + i * 512);
        else Ah[bf][i] = *(lp2)(size_t)(a_hi + u0 * 64 + i * 512);
        SCHED0;
      };
      static_for<0, 24>([&](auto jc) { ld(std::integral_constant<int, 0>{}, jc); });
      static_for<0, 4>([&](auto uc) {
        constexpr int u0 = decltype(uc)::value, cur = u0 & 1;
        static_for<0, 36>([&](auto nc) {
          constexpr int n = decltype(nc)::value;
          constexpr int u1 = tri_row(n), t = u1 + (n - tri_start(u1));
          bf16x8 A = __builtin_bit_cast(bf16x8, (u32x4){Al[cur][u1][0], Al[cur][u1][1], Ah[cur][u1][0], Ah[cur][u1][1]});
          acc[t] = __builtin_amdgcn_mfma_f32_16x16x32_bf16(A, G[cur][t - u1], acc[t], 0, 0, 0);
          SCHED0;
          if constexpr (u0 < 3 && n >= CVO && n < CVO + 24) ld(std::integral_constant<int, u0 + 1>{}, std::integral_constant<int, n - CVO>{});
          if constexpr (u0 == 3) {
            if constexpr (n < 4) kv[n] = *(lp2)(size_t)(kvo - n * 512);
            else if constexpr (n == 4) { mv = rbf((mp[0] + mp[1]) + mp[2]); asm volatile("" : "+v"(mv)); }
            else if constexpr (n == 5) { mv = rbf(mv + one); asm volatile("" : "+v"(mv)); }
            else if constexpr (n == 6) {
              mt = mv * __int_as_float(__builtin_amdgcn_update_dpp(0, __float_as_int(mv), 0x128, 0xf, 0xf, false));
              asm volatile("" : "+v"(mt));
            } else if constexpr (n >= 8 && n < 12) {
              constexpr int c = n - 8;
              cB[c] = __int_as_float(__builtin_amdgcn_readlane(__float_as_int(mt), c));
              cA[c] = __int_as_float(__builtin_amdgcn_readlane(__float_as_int(mt), 4 + c));
              cb1[c] = __int_as_float(__builtin_amdgcn_readlane(__float_as_int(mv), 16 + c));
              asm volatile("" : "+v"(cB[c]), "+s"(cA[c]), "+s"(cb1[c]));
            } else if constexpr (n >= 14 && n < 30) {
              constexpr int i = (n - 14) >> 2, c = (n - 14) & 3;
              xf[i][c] = (c & 1) ? bfhi(q4[i][c >> 1]) : bflo(q4[i][c >> 1]);
              hh[i][c] = fmaf(fmaf(xf[i][c], st1[i][0], st1[i][1]), cA[c], cB[c]);
              asm volatile("" : "+v"(xf[i][c]), "+v"(hh[i][c]));
            } else if constexpr (n == 31) {
              asm volatile("global_load_dword %0, %1, %2" : "=v"(wm) : "v"(wo), "s"(Wd) : "memory");
            } else if constexpr (n == 32) {
              asm volatile("global_load_dword %0, %1, %2" : "=v"(wm2) : "v"(wo2), "s"(hw ? Wu : Wg) : "memory");
            }
            SCHED0;
          }
        });
      });
    }
    STAMPV(2);
    auto fin = [&](auto hc) {
      constexpr int H = decltype(hc)::value;
      char* xb = smem + CB_X + wid * 4096 + lane * 16;
#pragma unroll
      for (int t = 0; t < 4; ++t) *(f32x4*)(xb + t * 1024) = acc[4 * (H ^ 1) + t];
      __syncthreads();
      const char* xr = smem + CB_X + (wid ^ 4) * 4096 + lane * 16;
      f32x4 o[4];
#pragma unroll
      for (int t = 0; t < 4; ++t) o[t] = *(const f32x4*)(xr + t * 1024);
      SCHED0;
#pragma unroll
      for (int t = 0; t < 4; ++t) {
        const f32x4 y = acc[4 * H + t];
        const float y0 = fmaf(bfhi(kv[t][1]), Dv, y[0]) + o[t][0], y1 = fmaf(bflo(kv[t][1]), Dv, y[1]) + o[t][1];
        const float y2 = fmaf(bfhi(kv[t][0]), Dv, y[2]) + o[t][2], y3 = fmaf(bflo(kv[t][0]), Dv, y[3]) + o[t][3];
        *(u32x2*)(smem + CB_Y + cw * YS + (256 * (H * 4 + t) + 16 * b + 4 * g) * 2) = (u32x2){pack2(y0, y1), pack2(y2, y3)};
      }
    };
    if (hw == 0) fin(std::integral_constant<int, 0>{});
    else fin(std::integral_constant<int, 1>{});
    __syncthreads();
    STAMPV(3);
    {
      const int q = (lane >> 2) & 3, p = lane & 3;
      const unsigned toff = q * YS + (wid * 64 + 16 * g + 4 * p) * 2;
      char* xo = ws + WS_X1F + (size_t)(c0 >> 4) * 1024 + ((c0 >> 3) & 1) * 512 + ((c0 >> 2) & 1) * 8;
      char* zo = ws + WS_XF + (size_t)(c0 >> 5) * 1024 + ((c0 >> 4) & 1) * 512 + ((c0 >> 2) & 1) * 256 + ((c0 >> 3) & 1) * 8;
      const unsigned vo = (tid >> 5) * 36 * 1024 + (tid & 31) * 16, vz = (tid >> 4) * 18 * 1024 + (tid & 15) * 16;
      u32x2 y4[4];
#pragma unroll
      for (int i = 0; i < 4; ++i) y4[i] = trread(lds0 + CB_Y + toff + i * 1024);
      SCHED0;
#pragma unroll
      for (int i = 0; i < 4; ++i) {
        float x1[4];
#pragma unroll
        for (int c = 0; c < 4; ++c) x1[c] = fmaf(hh[i][c], (c & 1) ? bfhi(y4[i][c >> 1]) : bflo(y4[i][c >> 1]), xf[i][c]);
        gstore8c(zo + i * (16 * 36 * 1024), vz, (u32x2){pack2(x1[0] * cb1[0], x1[1] * cb1[1]), pack2(x1[2] * cb1[2], x1[3] * cb1[3])});
        gstore8c(xo + i * (16 * 36 * 1024), vo, (u32x2){pack2(x1[0], x1[1]), pack2(x1[2], x1[3])});
      }
    }
    STAMPV(4);
    wait_vm<0>();
    keep(wm); keep(wm2);
    STAMPV(5); STAMPOUT(2, 6, bid);
  } else if (bid < NCONV + NUV) {
    STAMPDECL STAMPV(0);
#define UVSLEEP 16
    __builtin_amdgcn_s_sleep(UVSLEEP);
    const int ub = bid - NCONV;
    const int kp = ub / 36, cbk = ub % 36;
    const int mat = cbk / 18, col0 = (cbk % 18) * 256;
    const u16* W = mat ? Wu : Wg;
    const int cg = tid & 31, rg = tid >> 5;
    u32x4 wv[12];
    const float* mp = (const float*)(ws + WS_MODP) + 3 * D_ + (tid >= 192 ? D_ - 192 : 0) + kp * 192 + (tid < 384 ? tid : 383);
    const float m0 = mp[0], m1 = mp[3456], m2 = mp[2 * 3456];
    SCHED0;
#pragma unroll
    for (int i = 0; i < 12; ++i)
      wv[i] = *(const u32x4*)(W + (size_t)(kp * 192 + 2 * rg + (i & 1) + 32 * (i >> 1)) * F_ + col0 + cg * 8);
    SCHED0;
    const float mv = rbf((m0 + m1) + m2);
    ((u16*)smem)[tid] = (u16)pack2(tid >= 192 ? 1.f + mv : mv, 0.f);
    __syncthreads();
    float a1[8], a2[8];
#pragma unroll
    for (int j = 0; j < 8; ++j) { a1[j] = 0.f; a2[j] = 0.f; }
#pragma unroll
    for (int i = 0; i < 6; ++i) {
      const unsigned sbv = *(const unsigned*)(smem + (2 * rg + 32 * i) * 2);
      const unsigned c1 = *(const unsigned*)(smem + (192 + 2 * rg + 32 * i) * 2);
#pragma unroll
      for (int p = 0; p < 4; ++p) {
        unsigned lo = __builtin_amdgcn_perm(wv[2 * i + 1][p], wv[2 * i][p], 0x05040100u);
        unsigned hi = __builtin_amdgcn_perm(wv[2 * i + 1][p], wv[2 * i][p], 0x07060302u);
        a1[2 * p] = dot2(lo, c1, a1[2 * p]); a1[2 * p + 1] = dot2(hi, c1, a1[2 * p + 1]);
        a2[2 * p] = dot2(lo, sbv, a2[2 * p]); a2[2 * p + 1] = dot2(hi, sbv, a2[2 * p + 1]);
      }
    }
    float* red = (float*)(smem + 1024);
#pragma unroll
    for (int j = 0; j < 4; ++j)
      *(f32x4*)(red + rg * 512 + (j * 32 + cg) * 4) = (f32x4){a1[2 * j], a2[2 * j], a1[2 * j + 1], a2[2 * j + 1]};
    __syncthreads();
    {
      float r = 0.f;
#pragma unroll
      for (int w = 0; w < 16; ++w) r += red[w * 512 + tid];
      const int j = tid >> 7, cgo = (tid >> 2) & 31, e = tid & 3;
      const int f = col0 + cgo * 8 + 2 * j + (e >> 1);
      char* uo = ws + WS_UF + (f >> 4) * 1024 + mat * 512 + (kp * 16 + (f & 15)) * 8 + (e & 1) * 4;
      st4(uo, pack2(r, r - rbf(r)));
      if (kp == 2) st4(uo + 128, 0u);
    }
    STAMPV(1); STAMPOUT(4, 2, ub);
  } else {
    float g0 = modv(ws, 5 * D_ + tid), g1 = modv(ws, 5 * D_ + 512 + lane);
    st4((float*)(ws + WS_GB) + tid, __float_as_uint(g0));
    if (tid < 64) st4((float*)(ws + WS_GB) + 512 + tid, __float_as_uint(g1));
  }
  keep(pf);
}

template <int OFF> DI void gload16(bf16x8& dst, const char* sbase, unsigned voff) {
  asm volatile("global_load_dwordx4 %0, %1, %2 offset:%3" : "=v"(dst) : "v"(voff), "s"(sbase), "i"(OFF) : "memory");
}
template <int OFF> DI void gload16a(bf16x8& dst, const char* sbase, unsigned voff) {
  asm volatile("global_load_dwordx4 %0, %1, %2 offset:%3" : "=a"(dst) : "v"(voff), "s"(sbase), "i"(OFF) : "memory");
}
template <int OFF> DI void gload8_sc1(u32x2& dst, const char* sbase, unsigned voff) {
  asm volatile("global_load_dwordx2 %0, %1, %2 offset:%3 sc1" : "=v"(dst) : "v"(voff), "s"(sbase), "i"(OFF) : "memory");
}
template <int OFF> DI void gload16_sc1(u32x4& dst, const char* sbase, unsigned voff) {
  asm volatile("global_load_dwordx4 %0, %1, %2 offset:%3 sc1" : "=v"(dst) : "v"(voff), "s"(sbase), "i"(OFF) : "memory");
}
template <int OFF> DI void gstore16_sc1(char* sbase, unsigned voff, const u32x4& v) {
  asm volatile("global_store_dwordx4 %0, %1, %2 offset:%3 sc1\n\ts_nop 1" ::"v"(voff), "v"(v), "s"(sbase), "i"(OFF) : "memory");
}
DI void gstore8_sc1(char* sbase, unsigned voff, const u32x2& v) {
  asm volatile("global_store_dwordx2 %0, %1, %2 sc1" ::"v"(voff), "v"(v), "s"(sbase) : "memory");
}
DI void pin4(f32x4& v) { asm volatile("" : "+v"(v)); }
DI void pin4u(u32x4& v) { asm volatile("" : "+v"(v)); }
DI void pin2u(u32x2& v) { asm volatile("" : "+v"(v)); }
DI void pinb(bf16x8& v) { asm volatile("" : "+v"(v)); }
DI void pinba(bf16x8& v) { asm volatile("" : "+a"(v)); }
DI bf16x8 trfrag(unsigned addr_lo, unsigned addr_hi) {
  s16x4 lo = __builtin_amdgcn_ds_read_tr16_b64_v4i16((lds_s16x4p)(size_t)addr_lo);
  s16x4 hi = __builtin_amdgcn_ds_read_tr16_b64_v4i16((lds_s16x4p)(size_t)addr_hi);
  return __builtin_shufflevector(lo, hi, 0, 1, 2, 3, 4, 5, 6, 7);
}
template <int N> DI void wait_lgkm() { __builtin_amdgcn_s_waitcnt(0xC07F | (N << 8)); }
DI s16x4 trhalf(unsigned addr) { return __builtin_amdgcn_ds_read_tr16_b64_v4i16((lds_s16x4p)(size_t)addr); }
DI bf16x8 join(s16x4 lo, s16x4 hi) { return __builtin_shufflevector(lo, hi, 0, 1, 2, 3, 4, 5, 6, 7); }
template <int P> DI void dma_piece(const char* wsrc, const unsigned (&voff)[9], unsigned wdst) {
  dma1k<P * 1024>(wsrc, voff[P], wdst);
}
#define DMA9(SRC, DST) do { dma_piece<0>(SRC, voff, DST); dma_piece<1>(SRC, voff, DST); dma_piece<2>(SRC, voff, DST); \
    dma_piece<3>(SRC, voff, DST); dma_piece<4>(SRC, voff, DST); dma_piece<5>(SRC, voff, DST); \
    dma_piece<6>(SRC, voff, DST); dma_piece<7>(SRC, voff, DST); dma_piece<8>(SRC, voff, DST); } while (0)

constexpr int opc(int k, int t, int i) { return k * 10000 + t * 100 + i; }
constexpr int op_n(int op) { int k = op / 10000; return k == 0 ? 0 : (k == 2 || k == 6 || k == 8) ? 2 : k == 3 ? 3 : 1; }
constexpr int w3222(int t, int i, int f) {
  int first = (i == 0) ? 0 : 2 * i + 1, cnt = (i == 0) ? 3 : 2;
  return (f % 3 == 1 && f / 3 < cnt) ? opc(1, t, first + f / 3) : 0;
}
template <typename P> constexpr int since(int op, int s, int i) {
  int n = -1;
  for (int a = -1; a <= s; ++a)
    for (int b = 0; b < 4; ++b) {
      if (a == s && b == i) return n;
      for (int c = 0; c < 12; ++c) {
        int o = P::plan(a, b, c);
        if (n >= 0) n += op_n(o);
        if (o == op) n = 0;
      }
    }
  return n;
}
constexpr int cmin(int a, int b) { return a < b ? a : b; }

constexpr int G1_UP = 18432 + 128;
constexpr int SLOT1 = 37120;
constexpr int G1_UF = 4 * SLOT1;
constexpr int GEMM1_LDS = G1_UF + 9216;
#define NA 5
struct P1 {
  static constexpr int n(int op) { int k = op / 10000; return k == 0 ? 0 : k == 3 ? 3 : k == 6 ? 4 : k == 8 ? 2 : 1; }
  static constexpr int plan(int s, int i, int c) {
    if (s == -1) return i == 0 ? (c < 9 ? opc(1, 0, c) : c < 17 ? opc(2, 0, c - 9) : opc(7, 0, 0)) : 0;
    if (s == 0 && i == 0) return c < 9 ? opc(1, 1, c) : c < 17 ? opc(2, 1, c - 9) : opc(3, 0, 0);
    if (s > 5) return 0;
    if (c < 4) return i == 0 ? opc(2, s + 1, 4 + c) : opc(2, s + 2, c);
    if (s == 0) return c < 13 ? opc(1, 2, c - 4) : c < 16 ? opc(4, 0, c - 13) : 0;
    if (s == 1) return c < 13 ? opc(1, 3 + i, c - 4) : (i == 0 && c == 17) ? opc(5, 0, 0) : 0;
    if (s <= 4 && i == 0 && c % 3 == 0 && c < 13 && (s - 2) * 3 + c / 3 - 2 < 7) return opc(9, 0, (s - 2) * 3 + c / 3 - 2);
    if (s <= 4) return (c % 3 != 2) ? 0 : i == 0 ? opc(1, s + 3, c / 3 - 1) : c < 17 ? opc(1, s + 3, 4 + c / 3) : 0;
    if (i == 0) return c < 13 ? opc(1, 8, c - 4) : c < 17 ? opc(2, 7, c - 9) : 0;
    return c < 12 ? opc(2, 8, c - 4) : c == 12 ? opc(6, 0, 0) : c == 13 ? opc(7, 0, 1) : c == 14 ? opc(8, 0, 0) : 0;
  }
  static constexpr int since(int op, int s, int i, int c) {
    int r = -1;
    for (int a = -1; a <= s; ++a)
      for (int b = 0; b < 2; ++b)
        for (int d = 0; d < 18; ++d) {
          if (a == s && b == i && d == c) return r;
          int o = plan(a, b, d);
          if (r >= 0) r += n(o);
          if (o == op) r = 0;
        }
    return r;
  }
  static constexpr int CB = 12;
  static constexpr int wait1(int s, int i, int c) {
    if (c == 0) return since(opc(2, s, 4 * i + 3), s, i, 0);
    if (i == 1 && c == CB) return since(opc(1, s == 5 ? 7 : s + 1, 8), s, i, c);
    if (s == 1 && i == 0 && c == 9) return since(opc(3, 0, 0), s, i, c);
    return 63;
  }
  static constexpr int wait(int s, int i, int c) { return wait1(s, i, c); }
};
struct G1State {
  f32x4 acc[4][18];
  bf16x8 xr[4][4];
  bf16x8 xe[4];
  bf16x8 xt[2][4];
  bf16x8 wf[9];
  s16x4 lo;
  u32x2 cf[2];
  bf16x8 sv[3];
  u32x2 sg[4];
  float s1, s2;
  unsigned pf[4];
};
struct G1Ptr {
  const char *wbase, *xbase, *sx, *ufsrc, *sgsrc, *wdsrc;
  char *stdst, *pfill;
  unsigned voff[9], xoff[4];
  unsigned so, wdst0, lds0, sgoff;
  int wid, lane;
};
#define G1M(OP, CC, BC) asm volatile(OP " %0, %1, %2, %0" : "+" CC(c) : "v"(a), BC(b))
template <int J, int Z, int BV> DI void g1_mfma(f32x4& c, const bf16x8& a, const bf16x8& b) {
  if constexpr (Z) {
    if constexpr (J < 2 * NA) asm volatile("v_mfma_f32_16x16x32_bf16 %0, %1, %2, 0" : "=&a"(c) : "v"(a), "a"(b));
    else asm volatile("v_mfma_f32_16x16x32_bf16 %0, %1, %2, 0" : "=&v"(c) : "v"(a), "a"(b));
  } else if constexpr (BV) {
    if constexpr (J < 2 * NA) G1M("v_mfma_f32_16x16x32_bf16", "a", "v"); else G1M("v_mfma_f32_16x16x32_bf16", "v", "v");
  } else {
    if constexpr (J < 2 * NA) G1M("v_mfma_f32_16x16x32_bf16", "a", "a"); else G1M("v_mfma_f32_16x16x32_bf16", "v", "a");
  }
}
template <int J> DI void g1_mfmac(f32x4& c, const u32x2& a, const u32x2& b) {
  if constexpr (J < 2 * NA) G1M("v_mfma_f32_16x16x16_bf16", "a", "v"); else G1M("v_mfma_f32_16x16x16_bf16", "v", "v");
}
template <int I, int J, int H, int R> DI void g1_rd(G1State& st, unsigned rd) {
  s16x4 v = trhalf(rd + (J & 1) * G1_UP + (32 * I + 8 * H) * 288 + 32 * (J >> 1));
  if constexpr (H == 0) st.lo = v; else st.wf[R] = join(st.lo, v);
}
template <int Q, int H> DI void g1_rdl(G1State& st, const unsigned (&rd)[4], unsigned ufrd) {
  constexpr int ft = Q / 14, j = Q % 14;
  if constexpr (ft < 9) {
    if constexpr (j < 12) g1_rd<(j >> 1) & 1, 2 * ft + (j & 1), H, Q % 9>(st, rd[(2 + (j >> 2)) & 3]);
    else if constexpr (H == 0) st.cf[j & 1] = *(const __attribute__((address_space(3))) u32x2*)(size_t)(ufrd + ft * 1024 + (j & 1) * 512);
  }
}
DI float dpp_ror_add(float v, int) { return v; }
template <int CTRL> DI float dpp_add(float v) {
  return v + __int_as_float(__builtin_amdgcn_update_dpp(0, __float_as_int(v), CTRL, 0xf, 0xf, false));
}
DI float swap16_add(float v) {
  auto r = __builtin_amdgcn_permlane16_swap(__float_as_uint(v), __float_as_uint(v), false, false);
  return __uint_as_float(r[0]) + __uint_as_float(r[1]);
}
DI float swap32_add(float v) {
  auto r = __builtin_amdgcn_permlane32_swap(__float_as_uint(v), __float_as_uint(v), false, false);
  return __uint_as_float(r[0]) + __uint_as_float(r[1]);
}
template <int C> DI void g1_stats(G1State& st, const G1Ptr& p) {
  if constexpr (C < 3) {
    constexpr int j = (C + 2) % 3;
    if constexpr (C == 0) { st.s1 = 0.f; st.s2 = 0.f; }
    u32x4 t = __builtin_bit_cast(u32x4, st.sv[j]);
#pragma unroll
    for (int q = 0; q < 4; ++q) { st.s1 = dot2(t[q], 0x3f803f80u, st.s1); st.s2 = dot2(t[q], t[q], st.s2); }
    if constexpr (C == 0) { if (p.lane >= 16) { st.s1 = 0.f; st.s2 = 0.f; } }
  } else if constexpr (C == 3) {
    st.s1 = dpp_add<0x122>(st.s1); st.s2 = dpp_add<0x122>(st.s2);
    st.s1 = dpp_add<0x124>(st.s1); st.s2 = dpp_add<0x124>(st.s2);
  } else if constexpr (C == 4) {
    st.s1 = dpp_add<0x128>(st.s1); st.s2 = dpp_add<0x128>(st.s2);
  } else if constexpr (C == 5) {
    st.s1 = swap16_add(st.s1); st.s2 = swap16_add(st.s2);
  } else if constexpr (C == 6) {
    st.s1 = swap32_add(st.s1); st.s2 = swap32_add(st.s2);
  } else if constexpr (C == 7) {
    st.s1 = st.s1 * (1.f / D_);
    st.s2 = rbf(rbf(fmaxf(st.s2 * (1.f / D_) - st.s1 * st.s1, 0.f)) + 1e-5f);
  } else {
    const float sd = __builtin_amdgcn_sqrtf(st.s2);
    u32x2 gr = {pack2(-st.s1, -st.s1), pack2(sd, sd)};
    gstore8_sc1(p.stdst, (p.lane & 1) * 8, gr);
  }
}
constexpr size_t WSTEP1 = (size_t)64 * F_ * 2;
template <int S, int OP>
DI void g1_op(G1State& st, const G1Ptr& p) {
  constexpr int k = OP / 10000, t = (OP / 100) % 100, i = OP % 100;
  if constexpr (k == 1) {
    dma_piece<i>(p.wbase + t * WSTEP1, p.voff, p.wdst0 + (t & 3) * SLOT1);
  } else if constexpr (k == 2) {
    const char* xs = p.xbase + (t >> 1) * 4096;
    constexpr int off = (t & 1) * 2048 + (i >> 2) * 1024;
    if constexpr (t == 8) gload16<off>(st.xt[i >> 2][i & 3], xs, p.xoff[i & 3]);
    else if constexpr (t == 7 && i >= 4) gload16a<off>(st.xe[i & 3], xs, p.xoff[i & 3]);
    else gload16a<off>(st.xr[2 * (t & 1) + (i >> 2)][i & 3], xs, p.xoff[i & 3]);
  } else if constexpr (k == 3) {
    gload16<0>(st.sv[0], p.sx, p.so);
    gload16<0>(st.sv[1], p.sx, p.so + 16384);
    gload16<0>(st.sv[2], p.sx, (p.so & 4095) + 32768);
  } else if constexpr (k == 4) {
    int q = p.wid + 4 * i; q = q > 8 ? 8 : q;
    dma1k<G1_UF>(p.ufsrc + q * 1024, p.xoff[0], p.lds0 + q * 1024);
  } else if constexpr (k == 6) {
    gload8_sc1<0>(st.sg[0], p.sgsrc, p.sgoff); gload8_sc1<128>(st.sg[1], p.sgsrc, p.sgoff);
    gload8_sc1<256>(st.sg[2], p.sgsrc, p.sgoff); gload8_sc1<384>(st.sg[3], p.sgsrc, p.sgoff);
  } else if constexpr (k == 9) {
    const int dp = (i == 6) ? 8 : ((2 * p.wid + 2 + i) & 7);
    gstore16_sc1<0>(p.pfill + dp * 4096, p.xoff[0], (u32x4){~0u, ~0u, ~0u, ~0u});
  } else if constexpr (k == 8) {
    asm volatile("global_load_dword %0, %2, %4\n\tglobal_load_dword %1, %3, %4" : "=&v"(st.pf[2]), "=&v"(st.pf[3])
                 : "v"(p.xoff[0] * 8), "v"((p.lane < 16 ? p.lane : 16) * 128 + 8192), "s"(p.wdsrc) : "memory");
  } else if constexpr (k == 7) {
    if constexpr (i == 0) CODEPF(st.pf[0], "k_gemm1", "k_gemm2", p.wid, p.lane);
    else CODEPF(st.pf[1], "k_gemm2", "k_zend", p.wid, p.lane);
  }
}
template <int S>
DI void g1_super(G1State& st, const G1Ptr& p, const unsigned (&rd)[4]) {
  static_for<0, 36>([&](auto nc) {
    constexpr int n = decltype(nc)::value, i = n / 18, c = n % 18, xb = 2 * (S & 1) + i;
    if constexpr (P1::wait(S, i, c) < 63) wait_vm<P1::wait(S, i, c)>();
    if constexpr (c == 0) { pinba(st.xr[xb][0]); pinba(st.xr[xb][1]); pinba(st.xr[xb][2]); pinba(st.xr[xb][3]); }
    if constexpr (S == 1 && n == 9) { pinb(st.sv[0]); pinb(st.sv[1]); pinb(st.sv[2]); }
    if constexpr (n == 18 + P1::CB) {
      wait_lgkm<0>();
      asm volatile("" ::: "memory");
      __builtin_amdgcn_s_barrier();
      asm volatile("" ::: "memory");
    }
    if constexpr ((n & 1) == 0 && n <= 28) wait_lgkm<12>();
    SCHED0;
    static_for<0, 4>([&](auto mc) {
      constexpr int m = decltype(mc)::value;
      g1_mfma<c, (S == 0 && i == 0), 0>(st.acc[m][c], st.wf[n % 9], st.xr[xb][m]);
      if constexpr (n < 28) {
        if constexpr ((m & 1) == 0) g1_rd<(n + 8) / 18, (n + 8) % 18, m / 2, (n + 8) % 9>(st, rd[S & 3]);
      } else if constexpr (n >= 30 && (n < 32 || (m & 1) == 0)) {
        constexpr int k = n < 32 ? 2 * (n - 30) + m / 2 : n - 28, h = n < 32 ? (m & 1) : m / 2;
        if constexpr (S != 5) g1_rd<0, k, h, k>(st, rd[(S + 1) & 3]);
        else g1_rdl<k, h>(st, rd, 0);
      }
      if constexpr (m == 1) g1_op<S, P1::plan(S, i, c)>(st, p);
      if constexpr (m == 3 && S == 1 && n >= 9 && n < 18) g1_stats<n - 9>(st, p);
      SCHED0;
    });
  });
}

#define EPF 0
#define PS1(i) STAMPV(i)
#define PSF(i)
DI void hstore(char* sbase, unsigned voff, u32x4 v) {
  asm volatile("global_store_dwordx4 %0, %1, %2 sc1\n\ts_nop 1" ::"v"(voff), "v"(v), "s"(sbase) : "memory");
}
template <int F, int PH> DI void g1_ep(const f32x4& g, const f32x4& u, int r, float& g0, float& g1, float& u0, float& u1, unsigned& o) {
  if constexpr (F < NA) {
    if constexpr (PH == 0) asm volatile("v_accvgpr_read_b32 %0, %1" : "=v"(g0) : "a"(g[r]));
    else if constexpr (PH == 1) asm volatile("v_accvgpr_read_b32 %0, %2\n\tv_accvgpr_read_b32 %1, %3" : "=&v"(g1), "=&v"(u0) : "a"(g[r + 1]), "a"(u[r]));
    else if constexpr (PH == 2) asm volatile("v_accvgpr_read_b32 %0, %1" : "=v"(u1) : "a"(u[r + 1]));
    else if constexpr (PH == 3) asm volatile("v_max_i32 %0, 0, %0\n\tv_max_i32 %1, 0, %1" : "+v"(g0), "+v"(g1));
    else if constexpr (PH == 4) asm volatile("v_mul_f32 %0, %0, %1" : "+v"(g0) : "v"(u0));
    else asm volatile("v_mul_f32 %1, %1, %3\n\tv_cvt_pk_bf16_f32 %0, %2, %1" : "=v"(o), "+v"(g1) : "v"(g0), "v"(u1));
  } else {
    if constexpr (PH == 1) asm volatile("v_max_i32 %0, 0, %2\n\tv_max_i32 %1, 0, %3" : "=&v"(g0), "=&v"(g1) : "v"(g[r]), "v"(g[r + 1]));
    else if constexpr (PH == 3) asm volatile("v_mul_f32 %0, %0, %2\n\tv_mul_f32 %1, %1, %3" : "+v"(g0), "+v"(g1) : "v"(u[r]), "v"(u[r + 1]));
    else if constexpr (PH == 5) asm volatile("v_cvt_pk_bf16_f32 %0, %1, %2" : "=v"(o) : "v"(g0), "v"(g1));
  }
}

extern "C" __global__ __launch_bounds__(256, 1)
void k_gemm1(const u16* __restrict__ Wg, const u16* __restrict__ Wu, const u16* __restrict__ Wd, char* __restrict__ ws) {
  extern __shared__ __attribute__((aligned(16))) char smem[];
  const int tid = threadIdx.x, lane = tid & 63;
  const int wid = __builtin_amdgcn_readfirstlane(tid >> 6);
  const int bid = blockIdx.x;
  const int xcd = bid & 7, idx = bid >> 3;
  const int cb = (xcd >> 1) * 8 + (idx & 7), rb = (xcd & 1) * 4 + (idx >> 3);
  STAMPDECL STAMPV(0);
  G1Ptr p;
  p.lds0 = (unsigned)(size_t)(lds_cp)smem;
  p.wid = wid; p.lane = lane;
  p.wbase = (const char*)((wid & 2) ? Wu : Wg) + (size_t)(32 * (wid & 1)) * (F_ * 2) + cb * 288;
  p.xbase = ws + WS_XF + (size_t)(rb * 16 + wid * 4) * 18 * 1024;
#pragma unroll
  for (int i = 0; i < 9; ++i) { int c = 64 * i + lane; p.voff[i] = (c / 18) * (F_ * 2) + (c % 18) * 16; }
#pragma unroll
  for (int i = 0; i < 4; ++i) p.xoff[i] = lane * 16 + i * 18 * 1024;
  p.wdst0 = p.lds0 + (wid >> 1) * G1_UP + (wid & 1) * 9216;
  G1State st;
  static_for<0, 36>([&](auto cc) { g1_op<-1, P1::plan(-1, decltype(cc)::value / 18, decltype(cc)::value % 18)>(st, p); });
  SCHED0;
  const int srow = rb * 256 + cb * 8 + wid * 2;
  p.sx = ws + WS_X1F + (size_t)(srow >> 5) * 36 * 1024 + (srow & 31) * 16;
  p.so = (lane >> 1) * 512 + (lane & 1) * 16;
  p.stdst = ws + WS_ST2 + srow * 8;
  p.ufsrc = ws + WS_UF + (size_t)cb * 9 * 1024;
  p.sgsrc = ws + WS_ST2 + (rb * 256 + wid * 64) * 8;
  p.sgoff = (lane & 15) * 8;
  p.pfill = ws + WS_PART + (size_t)bid * 9 * 4096 + wid * 1024;
  p.wdsrc = (const char*)Wd + (size_t)(xcd >> 1) * (1152 * 1152) + (idx * 4 + wid) * (81 * 128);
  const int G = lane >> 4;
  const unsigned rd0 = p.lds0 + (16 * (G >> 1) + 4 * (G & 1) + ((lane >> 2) & 3)) * 288 + 8 * (lane & 3);
  unsigned rd[4] = {rd0, rd0 + SLOT1, rd0 + 2 * SLOT1, rd0 + 3 * SLOT1};
  asm volatile("" : "+v"(rd[0]), "+v"(rd[1]), "+v"(rd[2]), "+v"(rd[3]));
  SCHED0;
  wait_vm<P1::since(opc(1, 0, 8), 0, 0, 0)>();
  __builtin_amdgcn_s_barrier();
  SCHED0;
  STAMPV(1);
  static_for<0, 16>([&](auto fc) { constexpr int f = decltype(fc)::value; g1_rd<0, f / 2, f & 1, f / 2>(st, rd[0]); });
  SCHED0;
  g1_super<0>(st, p, rd);
  keep(st.pf[0]);
  PS1(2);
  g1_super<1>(st, p, rd);
  PS1(3);
  g1_super<2>(st, p, rd);
  g1_super<3>(st, p, rd);
  g1_super<4>(st, p, rd);
  g1_super<5>(st, p, rd);
  PS1(4);
  {
    char* hbase = ws + WS_HF + (size_t)(rb * 16 + wid * 4) * 144 * 1024;
    const unsigned hoff = (lane >> 5) * (144 * 1024) + (lane & 31) * 16 + cb * 9 * 512;
    const unsigned ufrd = p.lds0 + G1_UF + lane * 8;
    static_for<0, 10>([&](auto fc) {
      constexpr int f = decltype(fc)::value;
      u32x4 o;
      float g0, g1, u0, u1;
      static_for<0, 56>([&](auto nc) {
        constexpr int n = decltype(nc)::value;
        constexpr int j = n >> 2, m = n & 3, q = 14 * f + j, J = 2 * f + (j & 1);
        if constexpr (n == 0 && (f == 0 || f == 1 || f == 6 || f == 9)) PSF(f == 0 ? 1 : f == 1 ? 2 : f == 6 ? 3 : 4);
        if constexpr (f == 0 && n == 0) {
          wait_vm<P1::since(opc(1, 8, 8), 6, 0, 0)>();
#pragma unroll
          for (int i = 0; i < 4; ++i) { pinba(st.xr[0][i]); pinba(st.xr[1][i]); }
          __builtin_amdgcn_s_barrier();
          asm volatile("" ::: "memory");
          SCHED0;
        } else if constexpr (f == 0 && n == 16) {
          wait_vm<P1::since(opc(2, 7, 3), 6, 0, 0)>();
#pragma unroll
          for (int i = 0; i < 4; ++i) { pinba(st.xr[2][i]); pinba(st.xe[i]); }
          SCHED0;
        } else if constexpr (f == 0 && n == 32) {
          wait_vm<P1::since(opc(2, 8, 7), 6, 0, 0)>();
#pragma unroll
          for (int i = 0; i < 4; ++i) { pinb(st.xt[0][i]); pinb(st.xt[1][i]); }
          SCHED0;
        } else if constexpr (f == 0 && n == 48) {
          wait_vm<P1::since(opc(6, 0, 0), 6, 0, 0)>();
#pragma unroll
          for (int i = 0; i < 4; ++i) pin2u(st.sg[i]);
#pragma unroll 1
          for (int spin = 0; spin < (1 << 22); ++spin) {
            if (__builtin_amdgcn_ballot_w64(st.sg[0][1] == 0u || st.sg[1][1] == 0u || st.sg[2][1] == 0u || st.sg[3][1] == 0u) == 0ull) break;
            __builtin_amdgcn_s_sleep(2);
            g1_op<6, opc(6, 0, 0)>(st, p);
            wait_vm<0>();
#pragma unroll
            for (int i = 0; i < 4; ++i) pin2u(st.sg[i]);
          }
          SCHED0;
        }
        if constexpr (f < 9) {
          if constexpr ((n & 7) == 0 && q < 116) { wait_lgkm<10>(); SCHED0; }
          if constexpr (j < 6) g1_mfma<J, 0, 0>(st.acc[m][J], st.wf[q % 9], st.xr[j >> 1][m]);
          else if constexpr (j < 8) g1_mfma<J, 0, 0>(st.acc[m][J], st.wf[q % 9], st.xe[m]);
          else if constexpr (j < 12) g1_mfma<J, 0, 1>(st.acc[m][J], st.wf[q % 9], st.xt[(j >> 1) - 4][m]);
          else g1_mfmac<J>(st.acc[m][J], st.cf[j & 1], st.sg[m]);
          if constexpr ((m & 1) == 0) g1_rdl<q + 8, m / 2>(st, rd, ufrd);
          SCHED0;
        } else if constexpr (n == 0) {
          asm volatile("s_nop 15\n\ts_nop 7" ::: "memory");
          SCHED0;
        }
        if constexpr (f > EPF && n >= 2 && n < 54) {
          constexpr int e = f - 1, pr = (n - 2) / 26, t = (n - 2) % 26, em = 2 * pr + t / 12, d = (t / 6) & 1;
          if constexpr (t < 24) {
            unsigned od;
            g1_ep<e, t % 6>(st.acc[em][2 * e], st.acc[em][2 * e + 1], 2 * d, g0, g1, u0, u1, od);
            if constexpr (t % 6 == 5) o[t / 6] = od;
          } else if constexpr (t == 24) {
            asm volatile("s_nop 1\n\tv_permlane32_swap_b32 %0, %2\n\tv_permlane32_swap_b32 %1, %3" : "+v"(o[0]), "+v"(o[1]), "+v"(o[2]), "+v"(o[3]));
          } else {
            hstore(hbase + pr * 2 * 144 * 1024, hoff + e * 512, o);
          }
          if constexpr (f < 9) SCHED0;
        }
      });
    });
  }
  wait_vm<0>();
  keep(st.pf[1]); keep(st.pf[2]); keep(st.pf[3]);
  STAMPV(5); STAMPOUT(0, 6, bid);
}

#define XSL 1
constexpr int SLOT2 = 36864;
constexpr int GEMM2_LDS = 4 * SLOT2;
struct P2 {
  static constexpr int plan(int s, int i, int c) {
    if (s == -1) return (i < 3 && c < 4) ? opc(2, i, c) : 0;
    if (s <= 5) return c == 9 ? opc(2, s + 3, i) : 0;
    return 0;
  }
  static constexpr int wait(int s, int i) { return since<P2>(opc(2, s, i), s, i); }
};
struct G2State {
  f32x4 acc[2][9];
  bf16x8 xr[12][2];
  bf16x8 wf[2][9];
};
struct G2Ptr {
  const char* xbase;
  unsigned xoff0, xoff1;
  unsigned rd[4];
};
template <int Z> DI void mfma16(f32x4& c, const bf16x8& a, const bf16x8& b) {
  if constexpr (Z) asm volatile("v_mfma_f32_16x16x32_bf16 %0, %1, %2, 0" : "=&v"(c) : "v"(a), "v"(b));
  else asm volatile("v_mfma_f32_16x16x32_bf16 %0, %1, %2, %0" : "+v"(c) : "v"(a), "v"(b));
}
template <int I, int F> DI bf16x8 g2_frag(unsigned rd) {
  return trfrag(rd + (32 * I) * 288 + 32 * F, rd + (32 * I + 8) * 288 + 32 * F);
}
constexpr size_t WSTEP2 = (size_t)128 * D_ * 2;
template <int OP> DI void g2_op(G2State& st, const G2Ptr& p) {
  constexpr int k = OP / 10000, t = (OP / 100) % 100, i = OP % 100;
  if constexpr (k == 2) {
    gload16<i * 1024>(st.xr[4 * (t % 3) + i][0], p.xbase + t * 4096, p.xoff0);
    gload16<i * 1024>(st.xr[4 * (t % 3) + i][1], p.xbase + t * 4096, p.xoff1);
  }
}
template <int I, int F, int H> DI s16x4 g2_half(unsigned rd) { return trhalf(rd + (32 * I + 8 * H) * 288 + 32 * F); }
template <int S>
DI void g2_super(G2State& st, const G2Ptr& p) {
  constexpr int R = S % 3;
  static_for<0, 4>([&](auto ic) {
    constexpr int i = decltype(ic)::value;
    constexpr int cur = i & 1, nxt = cur ^ 1;
    wait_vm<P2::wait(S, i)>();
    pinb(st.xr[4 * R + i][0]); pinb(st.xr[4 * R + i][1]);
    if constexpr (i == 3) {
      asm volatile("" ::: "memory");
      __builtin_amdgcn_s_barrier();
      asm volatile("" ::: "memory");
    }
    SCHED0;
    static_for<0, 9>([&](auto fc) {
      constexpr int f = decltype(fc)::value;
      constexpr bool ld = (i < 3) || (S < 7) || (f >= 5);
      constexpr int li = (i < 3) ? i + 1 : (S < 7) ? 0 : (f >= 5 ? f - 5 : 0), lf = (i == 3 && S == 7) ? 8 : f;
      constexpr int ls = (i < 3) ? (S & 3) : ((S + 1) & 3), ld_to = (i == 3 && S == 7) ? li : f;
      if constexpr (f == 0) { wait_lgkm<8>(); SCHED0; }
      if constexpr (f == 5) { wait_lgkm<(i == 3 && S == 7) ? 0 : 10>(); SCHED0; }
      s16x4 lo;
      mfma16<(S == 0 && i == 0)>(st.acc[0][f], st.wf[cur][f], st.xr[4 * R + i][0]);
      if constexpr (ld) lo = g2_half<li, lf, 0>(p.rd[ls]);
      SCHED0;
      mfma16<(S == 0 && i == 0)>(st.acc[1][f], st.wf[cur][f], st.xr[4 * R + i][1]);
      if constexpr (ld) st.wf[nxt][ld_to] = join(lo, g2_half<li, lf, 1>(p.rd[ls]));
      SCHED0;
    });
    g2_op<P2::plan(S, i, 9)>(st, p);
    SCHED0;
  });
}
DI u32x4 g2_pack(const f32x4& a, const f32x4& b) {
  const unsigned t = 0xfffffffeu;
  return (u32x4){min(pack2(a[0], a[1]), t), min(pack2(a[2], a[3]), t), min(pack2(b[0], b[1]), t), min(pack2(b[2], b[3]), t)};
}
DI f32x4 g2_unpack(unsigned a, unsigned b) { return (f32x4){bflo(a), bfhi(a), bflo(b), bfhi(b)}; }
DI u32x2 g2_out(f32x4 s, float is2, const u32x2& x1, const f32x4& gb) {
  s = s * is2;
  float o0 = bflo(x1[0]) + rbf(gb[0] * rbf(s[0])), o1 = bfhi(x1[0]) + rbf(gb[1] * rbf(s[1]));
  float o2 = bflo(x1[1]) + rbf(gb[2] * rbf(s[2])), o3 = bfhi(x1[1]) + rbf(gb[3] * rbf(s[3]));
  return (u32x2){pack2(o0, o1), pack2(o2, o3)};
}
extern "C" __global__ __launch_bounds__(512, 1)
void k_gemm2(const u16* __restrict__ Wd, char* __restrict__ ws, u16* __restrict__ out) {
  extern __shared__ __attribute__((aligned(16))) char smem[];
  const int tid = threadIdx.x, lane = tid & 63;
  const int wid8 = __builtin_amdgcn_readfirstlane(tid >> 6);
  const int wid = wid8 & 3;
  const int bid = blockIdx.x;
  const int xcd = bid & 7, idx = bid >> 3;
  const int q = xcd >> 1;
  const int rb = (xcd & 1) * 8 + (idx >> 2);
  const int db = idx & 3;
  const unsigned lds0 = (unsigned)(size_t)(lds_cp)smem;
  if (wid8 >= 4) {
    const char* wsrc = (const char*)Wd + (size_t)(q * 1152 + 32 * wid) * (D_ * 2) + db * 288;
    unsigned voff[9];
#pragma unroll
    for (int i = 0; i < 9; ++i) {
      int c = 64 * i + lane, j = c % 18;
      voff[i] = (c / 18) * (D_ * 2) + (j < 16 ? ((j + 4 * q + 4) & 15) : j) * 16;
    }
    const unsigned wdst0 = lds0 + wid * 9216;
    unsigned pf;
    DMA9(wsrc, wdst0);
    CODEPF(pf, "k_gemm2", "k_zend", wid, lane);
    DMA9(wsrc + WSTEP2, wdst0 + SLOT2);
    wait_vm<9>();
    keep(pf);
    __builtin_amdgcn_s_barrier();
    DMA9(wsrc + 2 * WSTEP2, wdst0 + 2 * SLOT2);
    DMA9(wsrc + 3 * WSTEP2, wdst0 + 3 * SLOT2);
    wait_vm<18>();
    __builtin_amdgcn_s_barrier();
#pragma unroll 1
    for (int s = 1; s < 6; ++s) {
      wait_vm<9>();
      __builtin_amdgcn_s_barrier();
      DMA9(wsrc + (s + 3) * WSTEP2, wdst0 + ((s + 3) & 3) * SLOT2);
    }
    wait_vm<9>();
    __builtin_amdgcn_s_barrier();
    wait_vm<0>();
    __builtin_amdgcn_s_barrier();
    return;
  }
  STAMPDECL STAMPV(0);
  G2Ptr p;
  p.xbase = ws + WS_HF + ((size_t)(rb * 8 + wid * 2) * 144 + q * 36) * 1024;
  p.xoff0 = lane * 16; p.xoff1 = lane * 16 + 144 * 1024;
  G2State st;
  static_for<0, 3>([&](auto ic) {
    static_for<0, 4>([&](auto cc) { g2_op<P2::plan(-1, decltype(ic)::value, decltype(cc)::value)>(st, p); });
  });
  SCHED0;
  const int g = lane >> 4, ml = lane & 15;
  const unsigned rd0 = lds0 + (16 * (g >> 1) + 4 * (g & 1) + ((lane >> 2) & 3)) * 288 + 8 * (lane & 3);
#pragma unroll
  for (int i = 0; i < 4; ++i) p.rd[i] = rd0 + i * SLOT2;
  asm volatile("" : "+v"(p.rd[0]), "+v"(p.rd[1]), "+v"(p.rd[2]), "+v"(p.rd[3]));
  const int tw = (rb * 4 + db) * 4 + wid;
  char* pb = ws + WS_PART + (size_t)tw * 9 * 4096;
  const char* x1b = ws + WS_X1F + (size_t)(rb * 4 + wid) * 36 * 1024 + db * 9 * 1024;
  unsigned x1o = (g >> 1) * 512 + ml * 16 + (g & 1) * 8;
  unsigned gbo = (db * 144 + 4 * g) * 4;
  unsigned sgo = WS_ST2 + (rb * 128 + wid * 32 + ml) * 8 + 4;
  unsigned ob = ((rb * 128 + wid * 32 + ml) * D_ + db * 144 + 4 * g) * 2;
  pb = gpin(pb); x1b = gpin(x1b);
  asm volatile("" : "+v"(x1o), "+v"(gbo), "+v"(sgo), "+v"(ob));
  SCHED0;
  __builtin_amdgcn_s_barrier();
  SCHED0;
  STAMPV(1);
  static_for<0, 9>([&](auto fc) { constexpr int f = decltype(fc)::value; st.wf[0][f] = g2_frag<0, f>(p.rd[0]); });
  SCHED0;
  static_for<0, 8>([&](auto sc) { g2_super<decltype(sc)::value>(st, p); });
  const unsigned lo = lane * 16;
  wait_vm<0>();
#pragma unroll
  for (int i = 0; i < 4; ++i) { pinb(st.xr[8 + i][0]); pinb(st.xr[8 + i][1]); }
  SCHED0;
  u32x2 x1[5]; f32x4 gb[3]; unsigned sg[2];
  static_for<0, 9>([&](auto nc) {
    constexpr int n = decltype(nc)::value;
    constexpr int r = (n == 0) ? 8 : n - 1, rp = (n == 1) ? 8 : n - 2;
    constexpr int cur = n & 1, nxt = cur ^ 1;
    static_for<0, 4>([&](auto ic) {
      constexpr int i = decltype(ic)::value;
      s16x4 hl;
      mfma16<0>(st.acc[0][r], st.wf[cur][i], st.xr[8 + i][0]);
      if constexpr (n < 8) hl = g2_half<i, n, 0>(p.rd[0]);
      SCHED0;
      mfma16<0>(st.acc[1][r], st.wf[cur][i], st.xr[8 + i][1]);
      if constexpr (n < 8) st.wf[nxt][i] = join(hl, g2_half<i, n, 1>(p.rd[0]));
      SCHED0;
      if constexpr (n >= 1 && n <= 7 && i == 1) {
        pin4(st.acc[0][rp]); pin4(st.acc[1][rp]);
        const int dp = (rp == 8) ? 8 : ((2 * q + 2 + rp) & 7);
        gstore16_sc1<0>(pb + dp * 4096 + q * 1024, lo, g2_pack(st.acc[0][rp], st.acc[1][rp]));
        SCHED0;
      }
      if constexpr (n == 0) {
        if constexpr (i < 2) {
          x1[2 * i] = *(const u32x2*)(x1b + (2 * q + i) * 1024 + x1o);
          x1[2 * i + 1] = *(const u32x2*)(x1b + (2 * q + i) * 1024 + 256 + x1o);
          gb[i] = *(const f32x4*)(ws + WS_GB + (2 * q + i) * 64 + gbo);
        } else if constexpr (i == 2) {
          x1[4] = *(const u32x2*)(x1b + 8 * 1024 + (q & 1) * 256 + x1o);
          gb[2] = *(const f32x4*)(ws + WS_GB + 8 * 64 + gbo);
        } else {
          sg[0] = *(const unsigned*)(ws + sgo);
          sg[1] = *(const unsigned*)(ws + 128 + sgo);
        }
        SCHED0;
      }
    });
  });
  STAMPV(2);
  u32x4 pr[2][3]; u32x2 p8[3];
  unsigned long long mk[3];
  const char *pa[3], *pc[3];
#pragma unroll
  for (int k = 0; k < 3; ++k) {
    const int qs = (q + 1 + k) & 3;
    pa[k] = pb + (2 * q) * 4096 + qs * 1024 + 2048; pc[k] = pb + 8 * 4096 + qs * 1024 + (q & 1) * 8;
    mk[k] = ~0ull;
    asm volatile("" : "=v"(pr[0][k]), "=v"(pr[1][k]), "=v"(p8[k]));
  }
#pragma unroll 1
  for (int spin = 0; spin < (1 << 22); ++spin) {
#define XL(A, B, C, PA, PC, M) "s_mov_b64 exec, " M "\n\tglobal_load_dwordx4 " A ", %9, " PA " offset:-2048 sc1\n\t" \
    "global_load_dwordx4 " B ", %9, " PA " offset:2048 sc1\n\tglobal_load_dwordx2 " C ", %9, " PC " sc1\n\t"
    asm volatile(XL("%0", "%1", "%2", "%10", "%11", "%12") XL("%3", "%4", "%5", "%13", "%14", "%15")
                 XL("%6", "%7", "%8", "%16", "%17", "%18") "s_mov_b64 exec, -1\n\ts_waitcnt vmcnt(0)"
                 : "+v"(pr[0][0]), "+v"(pr[1][0]), "+v"(p8[0]), "+v"(pr[0][1]), "+v"(pr[1][1]), "+v"(p8[1]),
                   "+v"(pr[0][2]), "+v"(pr[1][2]), "+v"(p8[2])
                 : "v"(lo), "s"(pa[0]), "s"(pc[0]), "s"(mk[0]), "s"(pa[1]), "s"(pc[1]), "s"(mk[1]), "s"(pa[2]), "s"(pc[2]), "s"(mk[2])
                 : "memory");
#pragma unroll
    for (int k = 0; k < 3; ++k) {
      unsigned mx = max(p8[k][0], p8[k][1]);
#pragma unroll
      for (int i = 0; i < 4; ++i) mx = max(mx, max(pr[0][k][i], pr[1][k][i]));
      mk[k] = __builtin_amdgcn_ballot_w64(mx == ~0u);
    }
    if ((mk[0] | mk[1] | mk[2]) == 0ull) break;
    __builtin_amdgcn_s_sleep(XSL);
  }
  STAMPV(3); STAMPV(4);
  float is2[2];
#pragma unroll
  for (int m = 0; m < 2; ++m) { float s = bfhi(sg[m]); is2[m] = 1.f / (s * s); }
#pragma unroll
  for (int t = 6; t < 8; ++t) { pin4(st.acc[0][t]); pin4(st.acc[1][t]); }
#pragma unroll
  for (int m = 0; m < 2; ++m) {
    u32x2 o[2];
#pragma unroll
    for (int d = 0; d < 2; ++d) {
      f32x4 v = (st.acc[m][6 + d] + g2_unpack(pr[d][0][2 * m], pr[d][0][2 * m + 1])) +
                (g2_unpack(pr[d][1][2 * m], pr[d][1][2 * m + 1]) + g2_unpack(pr[d][2][2 * m], pr[d][2][2 * m + 1]));
      o[d] = g2_out(v, is2[m], x1[2 * d + m], gb[d]);
    }
    auto r0 = __builtin_amdgcn_permlane16_swap(o[0][0], o[1][0], false, false);
    auto r1 = __builtin_amdgcn_permlane16_swap(o[0][1], o[1][1], false, false);
    gstore16_sc1<0>((char*)out + m * 16 * D_ * 2 + (2 * q) * 32, ob + (g & 1) * 24, (u32x4){r0[0], r1[0], r0[1], r1[1]});
  }
  if (q < 2) {
    f32x4 own = q ? st.acc[1][8] : st.acc[0][8];
    f32x4 v = (own + g2_unpack(p8[0][0], p8[0][1])) + (g2_unpack(p8[1][0], p8[1][1]) + g2_unpack(p8[2][0], p8[2][1]));
    gstore8((char*)out, ob + q * 16 * D_ * 2 + 8 * 32, g2_out(v, q ? is2[1] : is2[0], x1[4], gb[2]));
  }
  STAMPDRAIN; STAMPV(5); STAMPOUT(1, 6, bid);
}

extern "C" __global__ void k_zend() {}

extern "C" void kernel_launch(void* const* d_in, const int* in_sizes, int n_in, void* d_out, int out_size,
                              void* d_ws, size_t ws_size, hipStream_t stream) {
  const u16* Aq = (const u16*)d_in[0];
  const u16* Ak = (const u16*)d_in[1];
  const u16* Av = (const u16*)d_in[2];
  const u16* shk = (const u16*)d_in[3];
  const u16* ssmk = (const u16*)d_in[4];
  const u16* ssmD = (const u16*)d_in[5];
  const u16* Wg = (const u16*)d_in[7];
  const u16* Wu = (const u16*)d_in[8];
  const u16* Wd = (const u16*)d_in[9];
  const u16* cvec = (const u16*)d_in[10];
  const u16* wmod = (const u16*)d_in[11];
  char* ws = (char*)d_ws;
  (void)hipFuncSetAttribute((const void*)k_conv, hipFuncAttributeMaxDynamicSharedMemorySize, CONV_LDS);
  (void)hipFuncSetAttribute((const void*)k_gemm1, hipFuncAttributeMaxDynamicSharedMemorySize, GEMM1_LDS);
  (void)hipFuncSetAttribute((const void*)k_gemm2, hipFuncAttributeMaxDynamicSharedMemorySize, GEMM2_LDS);
  k_pre<<<NMOD + NTILE + NSTAT, 256, 0, stream>>>(Aq, Ak, Av, shk, cvec, wmod, ssmk, ws);
  k_conv<<<NCONV + NUV + 1, 512, CONV_LDS, stream>>>(ssmk, ssmD, Wg, Wu, Wd, ws);
  k_gemm1<<<256, 256, GEMM1_LDS, stream>>>(Wg, Wu, Wd, ws);
  k_gemm2<<<256, 512, GEMM2_LDS, stream>>>(Wd, ws, (u16*)d_out);
}
```
